```python
import torch

def _softmax(x, axis=-1):
    return torch.softmax(x, dim=axis)

def _gelu(x):
    return torch.nn.functional.gelu(x, approximate='tanh')

def _silu(x):
    return torch.nn.functional.silu(x)

def _sigmoid(x):
    return torch.sigmoid(x)

def _layernorm(x, axis=-1, eps=1e-05):
    m = x.mean(dim=axis, keepdim=True)
    v = x.var(dim=axis, keepdim=True, unbiased=False)
    return (x - m) / torch.sqrt(v + eps)

def _scatter_add(tgt, idx, src, axis=0):
    return tgt.clone().index_add_(axis, idx, src)

def _segment_sum(src, seg, n):
    out = torch.zeros((n,) + src.shape[1:], dtype=src.dtype)
    return out.index_add_(0, seg, src)

def _pad(x, pw):
    flat = tuple((v for pair in reversed(pw) for v in pair))
    return torch.nn.functional.pad(x, flat)
kernel_name = 'interleave_layers__sliding_block__memory_aug_recurrent_mem_tr__prompt'

def setup_inputs():

    _w = lambda a: (lambda t: t.to(torch.bfloat16) if t.is_floating_point() else t)(torch.as_tensor(a))

    def _A_setup():
        B, S, D = (1, 2048, 448)
        return {'x': _w((lambda *s: torch.randn(*s, dtype=torch.bfloat16) * 0.05)(B, S, D)), 'wqkv': _w((lambda *s: torch.randn(*s, dtype=torch.bfloat16) * 0.05)(D, 3 * D)), 'wo': _w((lambda *s: torch.randn(*s, dtype=torch.bfloat16) * 0.05)(D, D))}
    _A_in = _A_setup()

    def _B_setup():
        B, S, Dm = (1, 2048, 448)
        H, Dh = (16, 256)
        M, seg = (8, 32)
        seg = min(seg, S)
        S = S // seg * seg
        return {'x': _w((lambda *s: torch.randn(*s, dtype=torch.bfloat16) * 0.05)(B, S, Dm)), 'mem0': _w((lambda *s: torch.randn(*s, dtype=torch.bfloat16) * 0.8)(B, M, Dm)), 'Wq': _w((lambda *s: torch.randn(*s, dtype=torch.bfloat16) * 1.6)(Dm, H, Dh) * Dm ** (-0.5)), 'Wk': _w((lambda *s: torch.randn(*s, dtype=torch.bfloat16) * 1.6)(Dm, H, Dh) * Dm ** (-0.5)), 'Wv': _w((lambda *s: torch.randn(*s, dtype=torch.bfloat16) * 0.8)(Dm, H, Dh) * Dm ** (-0.5)), 'Wo': _w((lambda *s: torch.randn(*s, dtype=torch.bfloat16) * 0.8)(H, Dh, Dm) * (H * Dh) ** (-0.5)), 'seg_len': seg}
    _B_in = _B_setup()
    return {'A_x': _A_in['x'], 'A_wqkv': _A_in['wqkv'], 'A_wo': _A_in['wo'], 'B_x': _B_in['x'], 'B_mem0': _B_in['mem0'], 'B_Wq': _B_in['Wq'], 'B_Wk': _B_in['Wk'], 'B_Wv': _B_in['Wv'], 'B_Wo': _B_in['Wo'], 'B_seg_len': _B_in['seg_len']}

def reference(A_x, A_wqkv, A_wo, B_x, B_mem0, B_Wq, B_Wk, B_Wv, B_Wo, B_seg_len):

    def _A_ref(x, wqkv, wo):
        B, S, D = x.shape
        H = 16
        Dh = D // H
        W = 256
        qkv = (lambda x, *d: x.permute(*d))((x @ wqkv).reshape(B, S, 3, H, Dh), 2, 0, 3, 1, 4)
        q, k, v = (qkv[0], qkv[1], qkv[2])
        scores = torch.einsum('bhqd,bhkd->bhqk', q, k) * Dh ** (-0.5)
        idx = torch.arange(S)
        mask = (idx[:, None] - idx[None, :] >= 0) & (idx[:, None] - idx[None, :] < W)
        scores = torch.where(mask[None, None], scores, -1000000000.0)
        p = _softmax(scores, axis=-1)
        o = torch.einsum('bhqk,bhkd->bhqd', p, v)
        return (lambda x, *d: x.permute(*d))(o, 0, 2, 1, 3).reshape(B, S, D) @ wo

    def _B_ref(x, mem0, Wq, Wk, Wv, Wo, seg_len):
        B, S, Dm = x.shape
        H, Dh = (Wq.shape[1], Wq.shape[2])
        M = mem0.shape[1]
        scale = Dh ** (-0.5)
        n_seg = S // seg_len
        L = M + seg_len
        idx = torch.arange(L)
        mask = (idx[:, None] < M) | (idx[:, None] >= idx[None, :])
        mem = mem0
        outs = []
        for s in range(n_seg):
            xs = x[:, s * seg_len:(s + 1) * seg_len, :].clone()
            h = torch.concatenate([mem, xs], axis=1)
            q = torch.einsum('btd,dhe->bhte', h, Wq)
            k = torch.einsum('btd,dhe->bhte', h, Wk)
            v = torch.einsum('btd,dhe->bhte', h, Wv)
            scores = torch.einsum('bhqe,bhke->bhqk', q, k) * scale
            scores = torch.where(mask[None, None], scores, -1000000000.0)
            p = _softmax(scores, axis=-1)
            o = torch.einsum('bhqk,bhke->bhqe', p, v)
            o = torch.einsum('bhte,hed->btd', o, Wo)
            mem = o[:, :M, :].clone()
            outs.append(o[:, M:, :])
        return torch.concatenate(outs, axis=1)
    x = A_x
    x = x + _A_ref(_layernorm(x, axis=-1), A_wqkv, A_wo)
    x = x + _B_ref(_layernorm(x, axis=-1), B_mem0, B_Wq, B_Wk, B_Wv, B_Wo, B_seg_len)
    x = x + _A_ref(_layernorm(x, axis=-1), A_wqkv, A_wo)
    x = x + _B_ref(_layernorm(x, axis=-1), B_mem0, B_Wq, B_Wk, B_Wv, B_Wo, B_seg_len)
    return x
```

```cpp
#include <hip/hip_runtime.h>
#include <hip/hip_bf16.h>
#include <stdint.h>

namespace {

typedef __bf16 bf16x2 __attribute__((ext_vector_type(2)));
typedef __bf16 bf16x8 __attribute__((ext_vector_type(8)));
typedef __bf16 bf16x16 __attribute__((ext_vector_type(16)));
typedef short s16x4 __attribute__((ext_vector_type(4)));
typedef float f32x4 __attribute__((ext_vector_type(4)));
typedef unsigned u32x2 __attribute__((ext_vector_type(2)));
typedef unsigned u32x4 __attribute__((ext_vector_type(4)));
typedef unsigned u32x8 __attribute__((ext_vector_type(8)));
#define LAS __attribute__((address_space(3)))
#define DI __device__ __forceinline__

constexpr int DM = 448, NSEG = 64, NT = 512;
constexpr unsigned SENT = 0xFFFFFFFFu;
constexpr unsigned SPIN_CAP = 1u << 17;
constexpr int ARM = 3;

constexpr unsigned OFF_CTRL = 0;
constexpr unsigned OFF_FA = 4096, SZ_FA = 16u * 28 * 14 * 1024;
constexpr unsigned OFF_FB = OFF_FA + SZ_FA;
constexpr unsigned OFF_FQ = OFF_FB + SZ_FA, SZ_FQ = 16u * 6 * 14 * 1024;
constexpr unsigned OFF_FW = OFF_FQ + SZ_FQ, SZ_FW = 28u * 14 * 1024;
constexpr unsigned OFF_ST = OFF_FW + SZ_FW;
constexpr unsigned XN_SZ = 28672, O_SZ = 32768, MEM_SZ = 8192, PM_SZ = 10240, PT_SZ = 40960, U_SZ = 458752;
constexpr unsigned L_XN = 0, L_XMID = L_XN + 64u * XN_SZ, L_XOUT = L_XMID + 64u * XN_SZ, L_O = L_XOUT + 64u * XN_SZ,
                   L_MEM = L_O + 64u * O_SZ, L_PM = L_MEM + 64u * MEM_SZ, L_PT = L_PM + 64u * PM_SZ, L_U = L_PT + 64u * PT_SZ,
                   L_TOTAL = L_U + 64u * U_SZ;
constexpr unsigned WS_NEED = OFF_ST + 2u * L_TOTAL;

struct Params {
    const uint16_t* x;
    const uint16_t* wqkv;
    const uint16_t* wo;
    const uint16_t* mem0;
    const uint16_t* Wq;
    const uint16_t* Wk;
    const uint16_t* Wv;
    const uint16_t* Wo;
    uint16_t* out;
    unsigned char* ws;
};

typedef __amdgpu_buffer_rsrc_t rsrc_t;
DI u32x4 ld16(rsrc_t r, unsigned off) { return __builtin_amdgcn_raw_buffer_load_b128(r, off, 0, 16); }
DI u32x2 ld8(rsrc_t r, unsigned off) { return __builtin_amdgcn_raw_buffer_load_b64(r, off, 0, 16); }
DI void st16(rsrc_t r, unsigned off, u32x4 v) { __builtin_amdgcn_raw_buffer_store_b128(v, r, off, 0, 16); }
DI void st8(rsrc_t r, unsigned off, u32x2 v) { __builtin_amdgcn_raw_buffer_store_b64(v, r, off, 0, 16); }
DI bool ok4(u32x4 v) { return (v.x != SENT) & (v.y != SENT) & (v.z != SENT) & (v.w != SENT); }
DI bool ok2(u32x2 v) { return (v.x != SENT) & (v.y != SENT); }
DI void drain() { asm volatile("s_waitcnt vmcnt(0)" ::: "memory"); }

DI bool spin_ok(unsigned& spins, rsrc_t ws) {
    asm volatile("" ::: "memory");
    ++spins;
    if ((spins & 63u) == 0u) {
        if (spins > SPIN_CAP) { __builtin_amdgcn_raw_buffer_store_b32(1u, ws, OFF_CTRL, 0, 16); return false; }
        const unsigned a = __builtin_amdgcn_raw_buffer_load_b32(ws, OFF_CTRL, 0, 16);
        if (__builtin_amdgcn_readfirstlane(a) != 0u) return false;
    }
    return true;
}

DI unsigned pk2(float a, float b) { bf16x2 v = {(__bf16)a, (__bf16)b}; return __builtin_bit_cast(unsigned, v); }
DI unsigned nosent(unsigned x) { return x < 0xFFFFFFFEu ? x : 0xFFFFFFFEu; }
DI u32x2 pk4(f32x4 v) { return (u32x2){pk2(v[0], v[1]), pk2(v[2], v[3])}; }
DI u32x2 pk4s(f32x4 v) { return (u32x2){nosent(pk2(v[0], v[1])), nosent(pk2(v[2], v[3]))}; }
DI float bflo(unsigned x) { return __builtin_bit_cast(float, x << 16); }
DI float bfhi(unsigned x) { return __builtin_bit_cast(float, x & 0xFFFF0000u); }
DI float bfr(float x) { return (float)(__bf16)x; }
DI bf16x8 as8(u32x4 v) { return __builtin_bit_cast(bf16x8, v); }
DI bf16x8 cat8(u32x2 a, u32x2 b) { return as8((u32x4){a.x, a.y, b.x, b.y}); }
DI bf16x16 cat16(u32x4 a, u32x4 b) { return __builtin_bit_cast(bf16x16, (u32x8){a.x, a.y, a.z, a.w, b.x, b.y, b.z, b.w}); }
DI f32x4 mfma(bf16x8 a, bf16x8 b, f32x4 c) { return __builtin_amdgcn_mfma_f32_16x16x32_bf16(a, b, c, 0, 0, 0); }
DI f32x4 smfma(bf16x8 a, bf16x16 b, f32x4 c, int idx) { return __builtin_amdgcn_smfmac_f32_16x16x64_bf16(a, b, c, idx, 0, 0); }
template <int CTRL> DI float dpp(float v) {
    return __builtin_bit_cast(float, __builtin_amdgcn_update_dpp(0, __builtin_bit_cast(int, v), CTRL, 0xf, 0xf, false));
}
DI float row16_max(float v) {
    v = fmaxf(v, dpp<0x128>(v)); v = fmaxf(v, dpp<0x124>(v)); v = fmaxf(v, dpp<0x122>(v)); v = fmaxf(v, dpp<0x121>(v)); return v;
}
DI float row16_sum(float v) {
    v += dpp<0x128>(v); v += dpp<0x124>(v); v += dpp<0x122>(v); v += dpp<0x121>(v); return v;
}
DI u32x4 lds16(const unsigned char* lds, unsigned off) { return *(const u32x4*)(lds + off); }
DI u32x2 lds8(const unsigned char* lds, unsigned off) { return *(const u32x2*)(lds + off); }
DI f32x4 ldsf4(const unsigned char* lds, unsigned off) { return *(const f32x4*)(lds + off); }
DI void sts16(unsigned char* lds, unsigned off, u32x4 v) { *(u32x4*)(lds + off) = v; }
DI void sts8(unsigned char* lds, unsigned off, u32x2 v) { *(u32x2*)(lds + off) = v; }
DI void sts4(unsigned char* lds, unsigned off, unsigned v) { *(unsigned*)(lds + off) = v; }
DI void stsf4(unsigned char* lds, unsigned off, f32x4 v) { *(f32x4*)(lds + off) = v; }
DI u32x2 lds_tr(unsigned char* lds, unsigned off) {
    return __builtin_bit_cast(u32x2, __builtin_amdgcn_ds_read_tr16_b64_v4i16((LAS s16x4*)(lds + off)));
}
constexpr float LOG2E = 1.4426950408889634f;
const u32x4 kS4 = {SENT, SENT, SENT, SENT};
const u32x2 kS2 = {SENT, SENT};

DI bool fetch_xn(rsrc_t ws, unsigned src, unsigned char* lds, unsigned xn_off, int tid) {
    const unsigned o0 = src + tid * 16u;
    const unsigned o3 = src + (1536u + (tid & 255)) * 16u;
    u32x4 v0, v1, v2, v3;
    for (unsigned spins = 0;;) {
        v0 = ld16(ws, o0); v1 = ld16(ws, o0 + 8192u); v2 = ld16(ws, o0 + 16384u); v3 = ld16(ws, o3);
        if (__all(ok4(v0) & ok4(v1) & ok4(v2) & ok4(v3))) break;
        if (!spin_ok(spins, ws)) return false;
    }
    sts16(lds, xn_off + tid * 16u, v0); sts16(lds, xn_off + tid * 16u + 8192u, v1); sts16(lds, xn_off + tid * 16u + 16384u, v2);
    if (tid < 256) sts16(lds, xn_off + (1536u + tid) * 16u, v3);
    return true;
}
template <bool WS_, bool WK_>
DI bool fetch_mem(rsrc_t ws, unsigned src, unsigned char* lds, unsigned ms_off, unsigned mk_off, int tid) {
    const int v = tid >> 5, q0 = (tid >> 2) & 7, rp = tid & 3;
    const int q = q0 == 7 ? 6 : q0;
    const unsigned off = src + (unsigned)(v * 512 + q * 64 + rp * 16);
    u32x4 m;
    for (unsigned spins = 0;;) {
        m = ld16(ws, off);
        if (__all(ok4(m))) break;
        if (!spin_ok(spins, ws)) return false;
    }
    if (q0 != 7) {
        const int Q = 7 * v + q;
        if (WK_) sts16(lds, mk_off + (unsigned)(Q * 64 + rp * 16), m);
        if (WS_) {
            const unsigned base = ms_off + (unsigned)((Q >> 4) * 1024 + ((Q >> 2) & 3) * 256 + (Q & 3) * 4 + rp * 32);
            sts4(lds, base, m.x); sts4(lds, base + 128, m.y); sts4(lds, base + 16, m.z); sts4(lds, base + 144, m.w);
        }
    }
    return true;
}
DI u32x2 xn_key(const unsigned char* lds, unsigned xn_off, int nt, int mt, int g, int r16) {
    return lds8(lds, xn_off + (unsigned)(((nt >> 1) * 2 + mt) * 1024 + ((2 * (nt & 1) + (g >> 1)) * 16 + r16) * 16 + (g & 1) * 8));
}
DI u32x2 mem_key(const unsigned char* lds, unsigned mk_off, int nt, int g, int r16) {
    return lds8(lds, mk_off + (unsigned)(((4 * nt + g) * 8 + (r16 & 7)) * 8));
}

DI void role_ms(const Params& p, rsrc_t ws, int L, int h, unsigned char* lds) {
    constexpr unsigned MS_OFF = 0, MK_OFF = 7168, SP_OFF = 14336, TT_OFF = 26624, XN_OFF = 43008, W_OFF = 71680;
    const int tid = threadIdx.x, w = __builtin_amdgcn_readfirstlane(tid >> 6), l = tid & 63, g = l >> 4, r16 = l & 15;
    const unsigned SB = OFF_ST + (unsigned)L * L_TOTAL;
    const u32x4* fa = (const u32x4*)(p.ws + OFF_FA + (size_t)h * 28 * 14 * 1024);
    bf16x16 wr[3][7];
#pragma unroll
    for (int t = 0; t < 3; ++t)
#pragma unroll
        for (int ks = 0; ks < 7; ++ks)
            wr[t][ks] = cat16(fa[((3 * w + t) * 14 + 2 * ks) * 64 + l], fa[((3 * w + t) * 14 + 2 * ks + 1) * 64 + l]);
#pragma unroll
    for (int k = 0; k < 7; ++k) sts16(lds, W_OFF + (tid + 512 * k) * 16u, fa[24 * 14 * 64 + tid + 512 * k]);
    const int sidx = (r16 & 8) ? 0xEEEE : 0x4444;
    const int nt3 = 24 + (w & 3);
    const unsigned ttb = TT_OFF + (unsigned)w * 2048u;
    const unsigned xk0 = w < 4 ? 0u : 4u;
    const unsigned xm = MS_OFF + xk0 * 1024u + l * 16u, xw = W_OFF + ((unsigned)(w & 3) * 14u + 2u * xk0) * 1024u + l * 16u;

    for (int s = 0; s < NSEG; ++s) {
        if (!fetch_xn(ws, SB + L_XN + (unsigned)s * XN_SZ, lds, XN_OFF, tid)) return;
        __syncthreads();
        if (!fetch_mem<true, true>(ws, SB + L_MEM + (unsigned)s * MEM_SZ, lds, MS_OFF, MK_OFF, tid)) return;
        __syncthreads();
        f32x4 acc[4];
#pragma unroll
        for (int t = 0; t < 4; ++t) acc[t] = (f32x4){0.f, 0.f, 0.f, 0.f};
#pragma unroll
        for (int ks = 0; ks < 7; ++ks) {
            const bf16x8 ma = as8(lds16(lds, MS_OFF + ks * 1024u + l * 16u));
#pragma unroll
            for (int t = 0; t < 3; ++t) acc[t] = smfma(ma, wr[t][ks], acc[t], sidx);
            if (ks < 3) {
                const unsigned o = xw + ks * 2048u;
                acc[3] = smfma(as8(lds16(lds, xm + ks * 1024u)), cat16(lds16(lds, o), lds16(lds, o + 1024)), acc[3], sidx);
            }
        }
        if (w < 4) acc[3] = smfma(as8(lds16(lds, xm + 3072u)), cat16(lds16(lds, xw + 6144u), lds16(lds, xw + 7168u)), acc[3], sidx);
        u32x2 tf[4];
#pragma unroll
        for (int t = 0; t < 4; ++t) sts8(lds, ttb + t * 512u + r16 * 32u + g * 8u, pk4(acc[t]));
#pragma unroll
        for (int t = 0; t < 4; ++t) tf[t] = lds_tr(lds, ttb + t * 512u + (4 * g + (r16 >> 2)) * 32u + (r16 & 3) * 8u);
        f32x4 sa[3];
#pragma unroll
        for (int k = 0; k < 3; ++k) sa[k] = (f32x4){0.f, 0.f, 0.f, 0.f};
#pragma unroll
        for (int pr = 0; pr < 2; ++pr) {
            const bf16x8 tb = cat8(tf[2 * pr], tf[2 * pr + 1]);
            const int na = 3 * w + 2 * pr, nb = pr == 0 ? 3 * w + 1 : nt3;
            sa[0] = mfma(cat8(mem_key(lds, MK_OFF, na, g, r16), mem_key(lds, MK_OFF, nb, g, r16)), tb, sa[0]);
            sa[1] = mfma(cat8(xn_key(lds, XN_OFF, na, 0, g, r16), xn_key(lds, XN_OFF, nb, 0, g, r16)), tb, sa[1]);
            sa[2] = mfma(cat8(xn_key(lds, XN_OFF, na, 1, g, r16), xn_key(lds, XN_OFF, nb, 1, g, r16)), tb, sa[2]);
        }
#pragma unroll
        for (int k = 0; k < 3; ++k) {
#pragma unroll
            for (int r = 0; r < 4; ++r) sa[k][r] += dpp<0x128>(sa[k][r]);
            if (r16 < 8) stsf4(lds, SP_OFF + (unsigned)(((w * 3 + k) * 32 + g * 8 + r16) * 16), sa[k]);
        }
        __syncthreads();
        if (w < 2) {
            const int i = 4 * w + g, q = r16;
            const int qq = q < 10 ? q : 9;
            const int kt = qq < 2 ? 0 : (qq < 6 ? 1 : 2), gq = qq < 2 ? qq : ((qq - 2) & 3);
            f32x4 sv = (f32x4){0.f, 0.f, 0.f, 0.f};
#pragma unroll
            for (int ww = 0; ww < 8; ++ww) sv += ldsf4(lds, SP_OFF + (unsigned)(((ww * 3 + kt) * 32 + gq * 8 + i) * 16));
            const float mx = row16_max(fmaxf(fmaxf(sv[0], sv[1]), fmaxf(sv[2], sv[3])));
            f32x4 e;
#pragma unroll
            for (int r = 0; r < 4; ++r) e[r] = __builtin_amdgcn_exp2f((sv[r] - mx) * LOG2E);
            const float part = q < 10 ? (e[0] + e[1]) + (e[2] + e[3]) : 0.f;
            const float inv = 1.0f / row16_sum(part);
            e *= inv;
            const unsigned po = (unsigned)h * 640u + (kt == 0 ? 512u + (unsigned)(gq * 8 + i) * 8u : (unsigned)(gq * 8 + i) * 16u + (unsigned)(kt - 1) * 8u);
            drain();
            if (q < 10) {
                st8(ws, SB + L_PM + (unsigned)s * PM_SZ + po, pk4s(e));
                if (s + ARM < NSEG) st8(ws, SB + L_PM + (unsigned)(s + ARM) * PM_SZ + po, kS2);
            }
        }
    }
}

DI void role_v(const Params& p, rsrc_t ws, int L, int v, unsigned char* lds) {
    constexpr unsigned MS_OFF = 0, Y_OFF = 7168, OP_OFF = 21504, W_OFF = 29696;
    const int tid = threadIdx.x, w = __builtin_amdgcn_readfirstlane(tid >> 6), l = tid & 63, g = l >> 4, r16 = l & 15;
    const unsigned SB = OFF_ST + (unsigned)L * L_TOTAL;
    const u32x4* fb = (const u32x4*)(p.ws + OFF_FB);
    auto wunit = [&](int tile, int kk, int gg, int n16) -> u32x4 {
        const int wc = tile * 16 + n16, hh = wc / 28, c = 28 * v + wc % 28;
        return fb[(((c >> 4) * 16 + hh) * 14 + kk) * 64 + gg * 16 + (c & 15)];
    };
    bf16x16 wr[3][7];
#pragma unroll
    for (int t = 0; t < 3; ++t)
#pragma unroll
        for (int ks = 0; ks < 7; ++ks) wr[t][ks] = cat16(wunit(3 * w + t, 2 * ks, g, r16), wunit(3 * w + t, 2 * ks + 1, g, r16));
#pragma unroll
    for (int k = 0; k < 7; ++k) {
        const int u = tid + 512 * k;
        sts16(lds, W_OFF + u * 16u, wunit(24 + u / 896, (u / 64) % 14, (u >> 4) & 3, u & 15));
    }
    const int sidx = (r16 & 8) ? 0xEEEE : 0x4444;
    const int ha = 2 * w, hb = 2 * w + 1;
    unsigned uoff[2]; unsigned yoff[2];
#pragma unroll
    for (int t = 0; t < 2; ++t) {
        int cl = 16 * t + r16; if (cl > 27) cl = 27;
        const int c = 28 * v + cl;
        uoff[t] = (unsigned)(((c >> 4) * 16 * 64 + g * 16 + (c & 15)) * 16);
        yoff[t] = Y_OFF + (unsigned)(cl * 32 + g * 8);
    }
    const unsigned ptok = (unsigned)((g * 8 + (r16 & 7)) * 16), pmem = 512u + (unsigned)(((g & 1) * 8 + (r16 & 7)) * 8);

    for (int s = 0; s < NSEG - 1; ++s) {
        u32x4 uf[2][2];
        {
            const unsigned ub = SB + L_U + (unsigned)s * U_SZ;
            for (unsigned spins = 0;;) {
#pragma unroll
                for (int t = 0; t < 2; ++t) { uf[0][t] = ld16(ws, ub + uoff[t] + ha * 1024u); uf[1][t] = ld16(ws, ub + uoff[t] + hb * 1024u); }
                if (__all(ok4(uf[0][0]) & ok4(uf[0][1]) & ok4(uf[1][0]) & ok4(uf[1][1]))) break;
                if (!spin_ok(spins, ws)) return;
            }
        }
        if (!fetch_mem<true, false>(ws, SB + L_MEM + (unsigned)s * MEM_SZ, lds, MS_OFF, 0, tid)) return;
        __syncthreads();
        f32x4 acc[4];
#pragma unroll
        for (int t = 0; t < 4; ++t) acc[t] = (f32x4){0.f, 0.f, 0.f, 0.f};
#pragma unroll
        for (int ks = 0; ks < 7; ++ks) {
            const bf16x8 ma = as8(lds16(lds, MS_OFF + ks * 1024u + l * 16u));
#pragma unroll
            for (int t = 0; t < 3; ++t) acc[t] = smfma(ma, wr[t][ks], acc[t], sidx);
        }
#pragma unroll
        for (int t = 0; t < 3; ++t) sts8(lds, Y_OFF + (unsigned)(((3 * w + t) * 16 + r16) * 32 + g * 8), pk4(acc[t]));
        if (w < 4) {
            asm volatile("" ::: "memory");
#pragma unroll
            for (int ks = 0; ks < 7; ++ks) {
                const unsigned o = W_OFF + (unsigned)((w * 14 + 2 * ks) * 1024 + l * 16);
                acc[3] = smfma(as8(lds16(lds, MS_OFF + ks * 1024u + l * 16u)), cat16(lds16(lds, o), lds16(lds, o + 1024)), acc[3], sidx);
            }
            sts8(lds, Y_OFF + (unsigned)(((24 + w) * 16 + r16) * 32 + g * 8), pk4(acc[3]));
        }
        __syncthreads();
        bf16x8 yf[2];
#pragma unroll
        for (int t = 0; t < 2; ++t) yf[t] = cat8(lds8(lds, yoff[t] + ha * 896u), lds8(lds, yoff[t] + hb * 896u));
        u32x4 pa, pb; u32x2 qa, qb;
        {
            const unsigned pbase = SB + L_PM + (unsigned)s * PM_SZ;
            for (unsigned spins = 0;;) {
                pa = ld16(ws, pbase + ha * 640u + ptok); pb = ld16(ws, pbase + hb * 640u + ptok);
                qa = ld8(ws, pbase + ha * 640u + pmem); qb = ld8(ws, pbase + hb * 640u + pmem);
                if (__all(ok4(pa) & ok4(pb) & ok2(qa) & ok2(qb))) break;
                if (!spin_ok(spins, ws)) return;
            }
        }
#pragma unroll
        for (int t = 0; t < 2; ++t) {
            f32x4 o = (f32x4){0.f, 0.f, 0.f, 0.f};
            o = mfma(as8(uf[0][t]), as8(pa), o);
            o = mfma(as8(uf[1][t]), as8(pb), o);
            o = mfma(yf[t], cat8(qa, qb), o);
            if (r16 < 8) stsf4(lds, OP_OFF + (unsigned)(((w * 2 + t) * 32 + g * 8 + r16) * 16), o);
        }
        __syncthreads();
        if (w == 0) {
            const int lc = l < 56 ? l : 55;
            f32x4 o = (f32x4){0.f, 0.f, 0.f, 0.f};
#pragma unroll
            for (int ww = 0; ww < 8; ++ww) o += ldsf4(lds, OP_OFF + (unsigned)((ww * 64 + lc) * 16));
            const unsigned mo = (unsigned)v * 512u + (unsigned)lc * 8u;
            drain();
            if (l < 56) {
                st8(ws, SB + L_MEM + (unsigned)(s + 1) * MEM_SZ + mo, pk4s(o));
                if (s + 1 + ARM < NSEG) st8(ws, SB + L_MEM + (unsigned)(s + 1 + ARM) * MEM_SZ + mo, kS2);
            }
        }
    }
}

DI void role_ts(const Params& p, rsrc_t ws, int L, int h, unsigned char* lds) {
    constexpr unsigned XN_OFF = 0, MK_OFF = 28672, SP_OFF = 35840, W_OFF = 76800;
    const int tid = threadIdx.x, w = __builtin_amdgcn_readfirstlane(tid >> 6), l = tid & 63, g = l >> 4, r16 = l & 15;
    const unsigned SB = OFF_ST + (unsigned)L * L_TOTAL;
    const u32x4* fa = (const u32x4*)(p.ws + OFF_FA + (size_t)h * 28 * 14 * 1024);
    bf16x8 wr[3][14];
#pragma unroll
    for (int t = 0; t < 3; ++t)
#pragma unroll
        for (int kk = 0; kk < 14; ++kk) wr[t][kk] = as8(fa[((3 * w + t) * 14 + kk) * 64 + l]);
#pragma unroll
    for (int k = 0; k < 7; ++k) sts16(lds, W_OFF + (tid + 512 * k) * 16u, fa[24 * 14 * 64 + tid + 512 * k]);
    const int nt3 = 24 + (w & 3);
    const unsigned xw = W_OFF + (unsigned)(w & 3) * 14336u + l * 16u, xx = XN_OFF + (unsigned)(w >> 2) * 1024u + l * 16u;

    for (int s = 0; s < NSEG; ++s) {
        if (!fetch_xn(ws, SB + L_XN + (unsigned)s * XN_SZ, lds, XN_OFF, tid)) return;
        __syncthreads();
        u32x2 tf[2][4];
#pragma unroll
        for (int mt = 0; mt < 2; ++mt) {
            f32x4 acc[3];
#pragma unroll
            for (int t = 0; t < 3; ++t) acc[t] = (f32x4){0.f, 0.f, 0.f, 0.f};
#pragma unroll
            for (int kk = 0; kk < 14; ++kk) {
                const bf16x8 xb = as8(lds16(lds, XN_OFF + (unsigned)((kk * 2 + mt) * 1024 + l * 16)));
#pragma unroll
                for (int t = 0; t < 3; ++t) acc[t] = mfma(wr[t][kk], xb, acc[t]);
            }
#pragma unroll
            for (int t = 0; t < 3; ++t) tf[mt][t] = pk4(acc[t]);
        }
        {
            f32x4 acc = (f32x4){0.f, 0.f, 0.f, 0.f};
#pragma unroll
            for (int kk = 0; kk < 14; ++kk)
                acc = mfma(as8(lds16(lds, xw + (unsigned)kk * 1024u)), as8(lds16(lds, xx + (unsigned)kk * 2048u)), acc);
            const u32x2 t3 = pk4(acc), z = {0u, 0u};
            tf[0][3] = w < 4 ? t3 : z; tf[1][3] = w < 4 ? z : t3;
        }
        f32x4 sa[5];
#pragma unroll
        for (int k = 0; k < 5; ++k) sa[k] = (f32x4){0.f, 0.f, 0.f, 0.f};
#pragma unroll
        for (int pr = 0; pr < 2; ++pr) {
            const int na = 3 * w + 2 * pr, nb = pr == 0 ? 3 * w + 1 : nt3;
            const bf16x8 k0 = cat8(xn_key(lds, XN_OFF, na, 0, g, r16), xn_key(lds, XN_OFF, nb, 0, g, r16));
            const bf16x8 k1 = cat8(xn_key(lds, XN_OFF, na, 1, g, r16), xn_key(lds, XN_OFF, nb, 1, g, r16));
            const bf16x8 t0 = cat8(tf[0][2 * pr], tf[0][2 * pr + 1]), t1 = cat8(tf[1][2 * pr], tf[1][2 * pr + 1]);
            sa[2] = mfma(k0, t0, sa[2]); sa[3] = mfma(k0, t1, sa[3]); sa[4] = mfma(k1, t1, sa[4]);
        }
        if (!fetch_mem<false, true>(ws, SB + L_MEM + (unsigned)s * MEM_SZ, lds, 0, MK_OFF, tid)) return;
        __syncthreads();
#pragma unroll
        for (int pr = 0; pr < 2; ++pr) {
            const int na = 3 * w + 2 * pr, nb = pr == 0 ? 3 * w + 1 : nt3;
            const bf16x8 km = cat8(mem_key(lds, MK_OFF, na, g, r16), mem_key(lds, MK_OFF, nb, g, r16));
            sa[0] = mfma(km, cat8(tf[0][2 * pr], tf[0][2 * pr + 1]), sa[0]);
            sa[1] = mfma(km, cat8(tf[1][2 * pr], tf[1][2 * pr + 1]), sa[1]);
        }
#pragma unroll
        for (int k = 0; k < 5; ++k) stsf4(lds, SP_OFF + (unsigned)(((w * 5 + k) * 64 + l) * 16), sa[k]);
        __syncthreads();
        {
            const int row = tid >> 4, q = tid & 15, mt = row >> 4, i = row & 15;
            const int qq = q < 10 ? q : 9;
            const int kt = qq < 2 ? 0 : (qq < 6 ? 1 : 2), gq = qq < 2 ? qq : ((qq - 2) & 3);
            const bool live = (q < 10) && !(kt == 2 && mt == 0);
            const int tile = kt == 0 ? mt : (kt == 1 ? 2 + mt : 4);
            f32x4 sv = (f32x4){0.f, 0.f, 0.f, 0.f};
#pragma unroll
            for (int ww = 0; ww < 8; ++ww) sv += ldsf4(lds, SP_OFF + (unsigned)(((ww * 5 + tile) * 64 + gq * 16 + i) * 16));
            bool lv[4];
#pragma unroll
            for (int r = 0; r < 4; ++r) {
                const int key = (kt - 1) * 16 + 4 * gq + r;
                lv[r] = live && (kt == 0 || key <= row);
                sv[r] = lv[r] ? sv[r] : -3.0e38f;
            }
            const float mx = row16_max(fmaxf(fmaxf(sv[0], sv[1]), fmaxf(sv[2], sv[3])));
            f32x4 e;
#pragma unroll
            for (int r = 0; r < 4; ++r) e[r] = lv[r] ? __builtin_amdgcn_exp2f((sv[r] - mx) * LOG2E) : 0.f;
            const float inv = 1.0f / row16_sum((e[0] + e[1]) + (e[2] + e[3]));
            e *= inv;
            const unsigned po = (unsigned)h * 2560u + (unsigned)mt * 1280u +
                                (kt == 0 ? 1024u + (unsigned)(gq * 16 + i) * 8u : (unsigned)(gq * 16 + i) * 16u + (unsigned)(kt - 1) * 8u);
            drain();
            if (q < 10) {
                st8(ws, SB + L_PT + (unsigned)s * PT_SZ + po, pk4s(e));
                if (s + ARM < NSEG) st8(ws, SB + L_PT + (unsigned)(s + ARM) * PT_SZ + po, kS2);
            }
        }
    }
}

DI void role_vu(const Params& p, rsrc_t ws, int L, int ct, unsigned char* lds) {
    constexpr unsigned XN_OFF = 0;
    const int tid = threadIdx.x, w = __builtin_amdgcn_readfirstlane(tid >> 6), l = tid & 63;
    const unsigned SB = OFF_ST + (unsigned)L * L_TOTAL;
    const u32x4* fb = (const u32x4*)(p.ws + OFF_FB + (size_t)ct * 16 * 14 * 1024);
    bf16x8 wr[2][14];
#pragma unroll
    for (int t = 0; t < 2; ++t)
#pragma unroll
        for (int kk = 0; kk < 14; ++kk) wr[t][kk] = as8(fb[((2 * w + t) * 14 + kk) * 64 + l]);
    for (int s = 0; s < NSEG; ++s) {
        const unsigned xo = XN_OFF + (unsigned)(s & 1) * XN_SZ;
        if (!fetch_xn(ws, SB + L_XN + (unsigned)s * XN_SZ, lds, xo, tid)) return;
        __syncthreads();
        f32x4 acc[2][2];
#pragma unroll
        for (int t = 0; t < 2; ++t) { acc[t][0] = (f32x4){0.f, 0.f, 0.f, 0.f}; acc[t][1] = (f32x4){0.f, 0.f, 0.f, 0.f}; }
#pragma unroll
        for (int kk = 0; kk < 14; ++kk)
#pragma unroll
            for (int mt = 0; mt < 2; ++mt) {
                const bf16x8 xa = as8(lds16(lds, xo + (unsigned)((kk * 2 + mt) * 1024 + l * 16)));
#pragma unroll
                for (int t = 0; t < 2; ++t) acc[t][mt] = mfma(xa, wr[t][kk], acc[t][mt]);
            }
        drain();
#pragma unroll
        for (int t = 0; t < 2; ++t) {
            const u32x2 a = pk4s(acc[t][0]), b = pk4s(acc[t][1]);
            const unsigned uo = (unsigned)((ct * 16 + 2 * w + t) * 1024 + l * 16);
            st16(ws, SB + L_U + (unsigned)s * U_SZ + uo, (u32x4){a.x, a.y, b.x, b.y});
            if (s + ARM < NSEG) st16(ws, SB + L_U + (unsigned)(s + ARM) * U_SZ + uo, kS4);
        }
    }
}

DI void role_vo(const Params& p, rsrc_t ws, int L, int ct, unsigned char* lds) {
    constexpr unsigned MS_OFF = 0, OP_OFF = 7168;
    const int tid = threadIdx.x, w = __builtin_amdgcn_readfirstlane(tid >> 6), l = tid & 63, g = l >> 4, r16 = l & 15;
    const unsigned SB = OFF_ST + (unsigned)L * L_TOTAL;
    const u32x4* fb = (const u32x4*)(p.ws + OFF_FB + (size_t)ct * 16 * 14 * 1024);
    bf16x16 wr[2][7];
#pragma unroll
    for (int t = 0; t < 2; ++t)
#pragma unroll
        for (int ks = 0; ks < 7; ++ks) wr[t][ks] = cat16(fb[((2 * w + t) * 14 + 2 * ks) * 64 + l], fb[((2 * w + t) * 14 + 2 * ks + 1) * 64 + l]);
    const int sidx = (r16 & 8) ? 0xEEEE : 0x4444;
    const int ha = 2 * w, hb = 2 * w + 1;
    const unsigned ptok = (unsigned)(l * 16), pmem = 1024u + (unsigned)(((g & 1) * 16 + r16) * 8);
    for (int s = 0; s < NSEG; ++s) {
        if (!fetch_mem<true, false>(ws, SB + L_MEM + (unsigned)s * MEM_SZ, lds, MS_OFF, 0, tid)) return;
        __syncthreads();
        f32x4 ya = (f32x4){0.f, 0.f, 0.f, 0.f}, yb = (f32x4){0.f, 0.f, 0.f, 0.f};
#pragma unroll
        for (int ks = 0; ks < 7; ++ks) {
            const bf16x8 ma = as8(lds16(lds, MS_OFF + ks * 1024u + l * 16u));
            ya = smfma(ma, wr[0][ks], ya, sidx); yb = smfma(ma, wr[1][ks], yb, sidx);
        }
        const bf16x8 yf = cat8(pk4(ya), pk4(yb));
        u32x4 ua, ub, pa[2], pb[2]; u32x2 qa[2], qb[2], xr;
        {
            const unsigned ubase = SB + L_U + (unsigned)s * U_SZ + (unsigned)(ct * 16 * 1024 + l * 16);
            const unsigned pbase = SB + L_PT + (unsigned)s * PT_SZ;
            const unsigned xoff = SB + L_XMID + (unsigned)s * XN_SZ + (unsigned)(ct * 1024 + ((w & 1) * 16 + r16) * 32 + g * 8);
            for (unsigned spins = 0;;) {
                ua = ld16(ws, ubase + ha * 1024u); ub = ld16(ws, ubase + hb * 1024u);
                bool ok = ok4(ua) & ok4(ub);
#pragma unroll
                for (int mt = 0; mt < 2; ++mt) {
                    pa[mt] = ld16(ws, pbase + ha * 2560u + mt * 1280u + ptok); pb[mt] = ld16(ws, pbase + hb * 2560u + mt * 1280u + ptok);
                    qa[mt] = ld8(ws, pbase + ha * 2560u + mt * 1280u + pmem); qb[mt] = ld8(ws, pbase + hb * 2560u + mt * 1280u + pmem);
                }
                xr = ld8(ws, xoff);
#pragma unroll
                for (int mt = 0; mt < 2; ++mt) ok = ok & ok4(pa[mt]) & ok4(pb[mt]) & ok2(qa[mt]) & ok2(qb[mt]);
                if (__all(ok & ok2(xr))) break;
                if (!spin_ok(spins, ws)) return;
            }
        }
#pragma unroll
        for (int mt = 0; mt < 2; ++mt) {
            f32x4 o = (f32x4){0.f, 0.f, 0.f, 0.f};
            o = mfma(as8(ua), as8(pa[mt]), o);
            o = mfma(as8(ub), as8(pb[mt]), o);
            o = mfma(yf, cat8(qa[mt], qb[mt]), o);
            stsf4(lds, OP_OFF + (unsigned)(((w * 2 + mt) * 64 + l) * 16), o);
        }
        __syncthreads();
        if (w < 2) {
            f32x4 o = (f32x4){0.f, 0.f, 0.f, 0.f};
#pragma unroll
            for (int ww = 0; ww < 8; ++ww) o += ldsf4(lds, OP_OFF + (unsigned)(((ww * 2 + w) * 64 + l) * 16));
            f32x4 r;
            r[0] = bflo(xr.x) + bfr(o[0]); r[1] = bfhi(xr.x) + bfr(o[1]); r[2] = bflo(xr.y) + bfr(o[2]); r[3] = bfhi(xr.y) + bfr(o[3]);
            const int row = w * 16 + r16;
            if (L == 0) {
                const unsigned xo = (unsigned)(ct * 1024 + row * 32 + g * 8);
                drain();
                st8(ws, SB + L_XOUT + (unsigned)s * XN_SZ + xo, pk4s(r));
                if (s + ARM < NSEG) st8(ws, SB + L_XOUT + (unsigned)(s + ARM) * XN_SZ + xo, kS2);
            } else {
                *(u32x2*)(p.out + (size_t)(s * 32 + row) * DM + ct * 16 + g * 4) = pk4(r);
            }
        }
    }
}

DI void role_wo(const Params& p, rsrc_t ws, int L, int mt, unsigned char* lds) {
    constexpr unsigned OI_OFF = 0, ROWB = 912, RED_OFF = OI_OFF + 16 * ROWB, W_OFF = 16384;
    const int tid = threadIdx.x, w = __builtin_amdgcn_readfirstlane(tid >> 6), l = tid & 63, g = l >> 4, r16 = l & 15;
    const unsigned SB = OFF_ST + (unsigned)L * L_TOTAL;
    const u32x4* fw = (const u32x4*)(p.ws + OFF_FW);
    bf16x8 wr[3][14];
#pragma unroll
    for (int t = 0; t < 3; ++t)
#pragma unroll
        for (int kk = 0; kk < 14; ++kk) wr[t][kk] = as8(fw[((3 * w + t) * 14 + kk) * 64 + l]);
#pragma unroll
    for (int k = 0; k < 7; ++k) sts16(lds, W_OFF + (tid + 512 * k) * 16u, fw[24 * 14 * 64 + tid + 512 * k]);
    const int ntile = w < 4 ? 4 : 3;
    float* red = (float*)(lds + RED_OFF);
    for (int s = 0; s < NSEG; ++s) {
        u32x4 ov[2]; u32x2 xr[4];
        {
            unsigned oo[2];
#pragma unroll
            for (int k = 0; k < 2; ++k) { const int u = tid + 512 * k; oo[k] = SB + L_O + (unsigned)s * O_SZ + (unsigned)((u >> 6) * 2048 + mt * 1024 + (u & 63) * 16); }
            if (L == 0) {
#pragma unroll
                for (int t = 0; t < 4; ++t) {
                    const int nt = t < 3 ? 3 * w + t : 24 + (w & 3);
                    xr[t] = *(const u32x2*)(p.x + (size_t)(s * 32 + mt * 16 + r16) * DM + nt * 16 + g * 4);
                }
            }
            const unsigned xb = OFF_ST + L_XOUT + (unsigned)s * XN_SZ + (unsigned)((mt * 16 + r16) * 32 + g * 8);
            for (unsigned spins = 0;;) {
                ov[0] = ld16(ws, oo[0]); ov[1] = ld16(ws, oo[1]);
                bool ok = ok4(ov[0]) & ok4(ov[1]);
                if (L != 0) {
#pragma unroll
                    for (int t = 0; t < 4; ++t) {
                        const int nt = t < 3 ? 3 * w + t : 24 + (w & 3);
                        xr[t] = ld8(ws, xb + (unsigned)nt * 1024u); ok = ok & ok2(xr[t]);
                    }
                }
                if (__all(ok)) break;
                if (!spin_ok(spins, ws)) return;
            }
        }
#pragma unroll
        for (int k = 0; k < 2; ++k) {
            const int u = tid + 512 * k, hh = u >> 6, row = (u >> 2) & 15, u4 = u & 3;
            const unsigned d = OI_OFF + (unsigned)(row * ROWB + hh * 56 + u4 * 16);
            sts8(lds, d, (u32x2){ov[k].x, ov[k].y});
            if (u4 != 3) sts8(lds, d + 8, (u32x2){ov[k].z, ov[k].w});
        }
        __syncthreads();
        f32x4 acc[4];
#pragma unroll
        for (int t = 0; t < 4; ++t) acc[t] = (f32x4){0.f, 0.f, 0.f, 0.f};
#pragma unroll
        for (int kk = 0; kk < 14; ++kk) {
            const bf16x8 ob = as8(lds16(lds, OI_OFF + (unsigned)(r16 * ROWB + kk * 64 + g * 16)));
#pragma unroll
            for (int t = 0; t < 3; ++t) acc[t] = mfma(wr[t][kk], ob, acc[t]);
            if (w < 4) acc[3] = mfma(as8(lds16(lds, W_OFF + (unsigned)((w * 14 + kk) * 1024 + l * 16))), ob, acc[3]);
        }
        float xm[4][4]; float sum = 0.f;
#pragma unroll
        for (int t = 0; t < 4; ++t) {
            xm[t][0] = bfr(bflo(xr[t].x) + bfr(acc[t][0])); xm[t][1] = bfr(bfhi(xr[t].x) + bfr(acc[t][1]));
            xm[t][2] = bfr(bflo(xr[t].y) + bfr(acc[t][2])); xm[t][3] = bfr(bfhi(xr[t].y) + bfr(acc[t][3]));
            if (t < ntile) sum += (xm[t][0] + xm[t][1]) + (xm[t][2] + xm[t][3]);
        }
        sum += __shfl_xor(sum, 16); sum += __shfl_xor(sum, 32);
        if (l < 16) red[w * 16 + l] = sum;
        __syncthreads();
        float mean = 0.f;
#pragma unroll
        for (int ww = 0; ww < 8; ++ww) mean += red[ww * 16 + r16];
        mean *= (1.0f / DM);
        float sq = 0.f;
#pragma unroll
        for (int t = 0; t < 4; ++t)
            if (t < ntile) {
#pragma unroll
                for (int r = 0; r < 4; ++r) { const float dlt = xm[t][r] - mean; sq += dlt * dlt; }
            }
        sq += __shfl_xor(sq, 16); sq += __shfl_xor(sq, 32);
        if (l < 16) red[128 + w * 16 + l] = sq;
        __syncthreads();
        float var = 0.f;
#pragma unroll
        for (int ww = 0; ww < 8; ++ww) var += red[128 + ww * 16 + r16];
        const float rstd = 1.0f / sqrtf(var * (1.0f / DM) + 1e-5f);
        drain();
#pragma unroll
        for (int t = 0; t < 4; ++t)
            if (t < ntile) {
                const int nt = t < 3 ? 3 * w + t : 24 + w;
                const f32x4 xv = {xm[t][0], xm[t][1], xm[t][2], xm[t][3]};
                const f32x4 nv = (xv - mean) * rstd;
                const unsigned mo = (unsigned)(nt * 1024 + (mt * 16 + r16) * 32 + g * 8);
                const unsigned no = (unsigned)(((nt >> 1) * 2 + mt) * 1024 + ((2 * (nt & 1) + (g >> 1)) * 16 + r16) * 16 + (g & 1) * 8);
                st8(ws, SB + L_XN + (unsigned)s * XN_SZ + no, pk4s(nv));
                st8(ws, SB + L_XMID + (unsigned)s * XN_SZ + mo, pk4s(xv));
                if (s + ARM < NSEG) {
                    st8(ws, SB + L_XN + (unsigned)(s + ARM) * XN_SZ + no, kS2);
                    st8(ws, SB + L_XMID + (unsigned)(s + ARM) * XN_SZ + mo, kS2);
                }
            }
    }
}

DI void role_at(const Params& p, rsrc_t ws, int L, int h, unsigned char* lds) {
    constexpr unsigned XA_OFF = 0, Q_OFF = 28672, RB = 80, K_OFF = Q_OFF + 32 * RB, V_OFF = K_OFF + 512 * RB, VRB = 1040,
                       MG_OFF = V_OFF + 32 * VRB;
    constexpr unsigned MGW = 2048 + 128;
    const int tid = threadIdx.x, w = __builtin_amdgcn_readfirstlane(tid >> 6), l = tid & 63, g = l >> 4, r16 = l & 15;
    const unsigned SB = OFF_ST + (unsigned)L * L_TOTAL;
    const u32x4* fq = (const u32x4*)(p.ws + OFF_FQ + (size_t)h * 6 * 14 * 1024);
    const int wt = w < 6 ? w : 5;
    bf16x8 wr[14];
#pragma unroll
    for (int kk = 0; kk < 14; ++kk) wr[kk] = as8(fq[(wt * 14 + kk) * 64 + l]);
    const int row = tid >> 4, pp = tid & 15;
    const int qt = w & 1, kq = w >> 1;
    const float scl = 0.18898223650461362f * LOG2E;

    for (int s = 0; s < NSEG; ++s) {
        u32x4 xv[4];
        if (L == 0) {
            const u32x4* xr = (const u32x4*)(p.x + (size_t)(s * 32 + row) * DM);
#pragma unroll
            for (int k = 0; k < 3; ++k) xv[k] = xr[pp + 16 * k];
            xv[3] = xr[48 + (pp & 7)];
        } else {
            const unsigned xb = OFF_ST + L_XOUT + (unsigned)s * XN_SZ + (unsigned)row * 32u;
            unsigned xo[4];
#pragma unroll
            for (int k = 0; k < 4; ++k) { const int u = k < 3 ? pp + 16 * k : 48 + (pp & 7); xo[k] = xb + (unsigned)((u >> 1) * 1024 + (u & 1) * 16); }
            for (unsigned spins = 0;;) {
#pragma unroll
                for (int k = 0; k < 4; ++k) xv[k] = ld16(ws, xo[k]);
                if (__all(ok4(xv[0]) & ok4(xv[1]) & ok4(xv[2]) & ok4(xv[3]))) break;
                if (!spin_ok(spins, ws)) return;
            }
        }
        {
            float f[4][8]; float sum = 0.f;
#pragma unroll
            for (int k = 0; k < 4; ++k) {
#pragma unroll
                for (int j = 0; j < 4; ++j) { f[k][2 * j] = bflo(xv[k][j]); f[k][2 * j + 1] = bfhi(xv[k][j]); }
                float ps = 0.f;
#pragma unroll
                for (int j = 0; j < 8; ++j) ps += f[k][j];
                sum += (k < 3 || pp < 8) ? ps : 0.f;
            }
            const float mean = row16_sum(sum) * (1.0f / DM);
            float sq = 0.f;
#pragma unroll
            for (int k = 0; k < 4; ++k) {
                float ps = 0.f;
#pragma unroll
                for (int j = 0; j < 8; ++j) { f[k][j] -= mean; ps += f[k][j] * f[k][j]; }
                sq += (k < 3 || pp < 8) ? ps : 0.f;
            }
            const float rstd = 1.0f / sqrtf(row16_sum(sq) * (1.0f / DM) + 1e-5f);
#pragma unroll
            for (int k = 0; k < 4; ++k) {
                const int u = k < 3 ? pp + 16 * k : 48 + (pp & 7);
                u32x4 o;
#pragma unroll
                for (int j = 0; j < 4; ++j) o[j] = pk2(f[k][2 * j] * rstd, f[k][2 * j + 1] * rstd);
                if (k < 3 || pp < 8)
                    sts16(lds, XA_OFF + (unsigned)(((u >> 2) * 2 + (row >> 4)) * 1024 + ((u & 3) * 16 + (row & 15)) * 16), o);
            }
        }
        __syncthreads();
        const int slot = s & 15;
        if (w < 6) {
            f32x4 acc[2];
            acc[0] = (f32x4){0.f, 0.f, 0.f, 0.f}; acc[1] = (f32x4){0.f, 0.f, 0.f, 0.f};
            if (w < 4) {
#pragma unroll
                for (int kk = 0; kk < 14; ++kk)
#pragma unroll
                    for (int mt = 0; mt < 2; ++mt)
                        acc[mt] = mfma(wr[kk], as8(lds16(lds, XA_OFF + (unsigned)((kk * 2 + mt) * 1024 + l * 16))), acc[mt]);
#pragma unroll
                for (int mt = 0; mt < 2; ++mt) {
                    const unsigned base = w < 2 ? Q_OFF : K_OFF + (unsigned)slot * 32u * RB;
                    sts8(lds, base + (unsigned)((mt * 16 + r16) * RB + (w & 1) * 32 + g * 8), pk4(acc[mt]));
                }
            } else {
#pragma unroll
                for (int kk = 0; kk < 14; ++kk)
#pragma unroll
                    for (int mt = 0; mt < 2; ++mt)
                        acc[mt] = mfma(as8(lds16(lds, XA_OFF + (unsigned)((kk * 2 + mt) * 1024 + l * 16))), wr[kk], acc[mt]);
                const u32x2 a = pk4(acc[0]), b = pk4(acc[1]);
                sts16(lds, V_OFF + (unsigned)(((w & 1) * 16 + r16) * VRB + slot * 64 + g * 16), (u32x4){a.x, a.y, b.x, b.y});
            }
        }
        __syncthreads();
        {
            const bf16x8 qf = as8(lds16(lds, Q_OFF + (unsigned)((qt * 16 + r16) * RB + g * 16)));
            int qd = qt * 16 + r16 - 4 * g + 32 * kq;
            asm volatile("" : "+v"(qd));
            f32x4 sc[3][2];
            float mx = -3.0e38f;
#pragma unroll
            for (int jj = 0; jj < 3; ++jj) {
                const int j = kq + 4 * jj;
                const bool segok = (j <= 8) && (j <= s);
                const int sl = (s - j) & 15;
#pragma unroll
                for (int kt = 0; kt < 2; ++kt) {
                    f32x4 c = (f32x4){0.f, 0.f, 0.f, 0.f};
                    if (segok) {
                        const bf16x8 kf = as8(lds16(lds, K_OFF + (unsigned)((sl * 32 + kt * 16 + r16) * RB + g * 16)));
                        c = mfma(kf, qf, c);
                    }
#pragma unroll
                    for (int r = 0; r < 4; ++r) {
                        const bool ok = segok && (jj == 0 ? qd >= kt * 16 + r : (jj == 1 ? true : qd < kt * 16 + r));
                        c[r] = ok ? c[r] * scl : -3.0e38f;
                        mx = fmaxf(mx, c[r]);
                    }
                    sc[jj][kt] = c;
                }
            }
            mx = fmaxf(mx, __shfl_xor(mx, 16)); mx = fmaxf(mx, __shfl_xor(mx, 32));
            float sum = 0.f;
            f32x4 o[2];
            o[0] = (f32x4){0.f, 0.f, 0.f, 0.f}; o[1] = (f32x4){0.f, 0.f, 0.f, 0.f};
#pragma unroll
            for (int jj = 0; jj < 3; ++jj) {
                const int j = kq + 4 * jj;
                const bool segok = (j <= 8) && (j <= s);
                const int sl = (s - j) & 15;
                f32x4 e[2];
#pragma unroll
                for (int kt = 0; kt < 2; ++kt)
#pragma unroll
                    for (int r = 0; r < 4; ++r) {
                        e[kt][r] = sc[jj][kt][r] > -1.0e38f ? __builtin_amdgcn_exp2f(sc[jj][kt][r] - mx) : 0.f;
                        sum += e[kt][r];
                    }
                if (segok) {
                    const bf16x8 pf = cat8(pk4(e[0]), pk4(e[1]));
#pragma unroll
                    for (int dt = 0; dt < 2; ++dt)
                        o[dt] = mfma(as8(lds16(lds, V_OFF + (unsigned)((dt * 16 + r16) * VRB + sl * 64 + g * 16))), pf, o[dt]);
                }
            }
            sum += __shfl_xor(sum, 16); sum += __shfl_xor(sum, 32);
            const unsigned mb = MG_OFF + (unsigned)w * MGW;
            stsf4(lds, mb + l * 16u, o[0]); stsf4(lds, mb + 1024u + l * 16u, o[1]);
            if (l < 16) { *(float*)(lds + mb + 2048 + l * 8) = mx; *(float*)(lds + mb + 2048 + l * 8 + 4) = sum; }
        }
        __syncthreads();
        if (w < 4) {
            const int dt = w >> 1;
            float m4[4], l4[4]; float M = -3.0e38f;
#pragma unroll
            for (int k = 0; k < 4; ++k) {
                const unsigned mb = MG_OFF + (unsigned)(2 * k + qt) * MGW + 2048u + r16 * 8u;
                m4[k] = *(const float*)(lds + mb); l4[k] = *(const float*)(lds + mb + 4); M = fmaxf(M, m4[k]);
            }
            float lsum = 0.f; f32x4 o = (f32x4){0.f, 0.f, 0.f, 0.f};
#pragma unroll
            for (int k = 0; k < 4; ++k) {
                const float f = m4[k] > -1.0e38f ? __builtin_amdgcn_exp2f(m4[k] - M) : 0.f;
                lsum += l4[k] * f;
                o += ldsf4(lds, MG_OFF + (unsigned)(2 * k + qt) * MGW + (unsigned)(dt * 1024 + l * 16)) * f;
            }
            o *= (1.0f / lsum);
            if (dt == 1 && g == 3) o = (f32x4){0.f, 0.f, 0.f, 0.f};
            const unsigned oo = (unsigned)(h * 2048 + (qt * 16 + r16) * 64 + dt * 32 + g * 8);
            drain();
            st8(ws, SB + L_O + (unsigned)s * O_SZ + oo, pk4s(o));
            if (s + ARM < NSEG) st8(ws, SB + L_O + (unsigned)(s + ARM) * O_SZ + oo, kS2);
        }
    }
}

__global__ void __launch_bounds__(NT, 2) k_main(Params p) {
    extern __shared__ __attribute__((aligned(16))) unsigned char lds[];
    const rsrc_t ws = __builtin_amdgcn_make_buffer_rsrc(p.ws, 0, WS_NEED, 0x00020000);
    const int b = blockIdx.x, x = b & 7, y = b >> 3;
    if (x < 2) {
        if (y < 16) role_ms(p, ws, x, y, lds); else role_v(p, ws, x, y - 16, lds);
        return;
    }
    int z = (x - 2) * 32 + y;
    if (z >= 180) return;
    const int L = z >= 90 ? 1 : 0; z -= L * 90;
    if (z < 16) role_at(p, ws, L, z, lds);
    else if (z < 18) role_wo(p, ws, L, z - 16, lds);
    else if (z < 34) role_ts(p, ws, L, z - 18, lds);
    else if (z < 62) role_vu(p, ws, L, z - 34, lds);
    else role_vo(p, ws, L, z - 62, lds);
}

__global__ void __launch_bounds__(NT, 2) k_pro(Params p) {
    extern __shared__ __attribute__((aligned(16))) unsigned char lds[];
    const int b = blockIdx.x, tid = threadIdx.x, w = __builtin_amdgcn_readfirstlane(tid >> 6), l = tid & 63, g = l >> 4, r16 = l & 15;
    if (b < 224) {
        const bool isB = b >= 112;
        const int bb = isB ? b - 112 : b, h = bb / 7, nb = bb % 7;
        constexpr unsigned RS = 528;
        if (isB) {
#pragma unroll
            for (int k = 0; k < 4; ++k) {
                const int u = tid + 512 * k, e = u >> 3, c8 = u & 7;
                const u32x4 v = *(const u32x4*)(p.Wo + ((size_t)h * 256 + e) * DM + nb * 64 + c8 * 8);
#pragma unroll
                for (int j = 0; j < 4; ++j) {
                    *(uint16_t*)(lds + (c8 * 8 + 2 * j) * RS + e * 2) = (uint16_t)(v[j] & 0xFFFFu);
                    *(uint16_t*)(lds + (c8 * 8 + 2 * j + 1) * RS + e * 2) = (uint16_t)(v[j] >> 16);
                }
            }
        } else {
#pragma unroll
            for (int k = 0; k < 4; ++k) {
                const int u = tid + 512 * k, n = u >> 5, e8 = u & 31;
                sts16(lds, n * RS + e8 * 16, *(const u32x4*)(p.Wk + ((size_t)(nb * 64 + n) * 16 + h) * 256 + e8 * 8));
            }
        }
        __syncthreads();
        if (w < 7) {
            const uint16_t* Wd = isB ? p.Wv : p.Wq;
            f32x4 acc[4][4];
#pragma unroll
            for (int i = 0; i < 4; ++i)
#pragma unroll
                for (int j = 0; j < 4; ++j) acc[i][j] = (f32x4){0.f, 0.f, 0.f, 0.f};
#pragma unroll
            for (int half = 0; half < 2; ++half) {
                u32x4 af[4][4];
#pragma unroll
                for (int ke = 0; ke < 4; ++ke)
#pragma unroll
                    for (int i = 0; i < 4; ++i)
                        af[ke][i] = *(const u32x4*)(Wd + ((size_t)(w * 64 + i * 16 + r16) * 16 + h) * 256 + (half * 4 + ke) * 32 + g * 8);
#pragma unroll
                for (int ke = 0; ke < 4; ++ke)
#pragma unroll
                    for (int j = 0; j < 4; ++j) {
                        const bf16x8 bfr_ = as8(lds16(lds, (unsigned)((j * 16 + r16) * RS + (half * 4 + ke) * 64 + g * 16)));
#pragma unroll
                        for (int i = 0; i < 4; ++i) acc[i][j] = mfma(as8(af[ke][i]), bfr_, acc[i][j]);
                    }
            }
            const float sc = isB ? 1.0f : 0.0625f;
#pragma unroll
            for (int i = 0; i < 4; ++i)
#pragma unroll
                for (int j = 0; j < 4; ++j) {
                    const int nt = nb * 4 + j, d0 = w * 64 + i * 16 + 4 * g, kk = d0 >> 5;
                    const size_t tile = isB ? (size_t)(nt * 16 + h) : (size_t)(h * 28 + nt);
                    unsigned char* dst = p.ws + (isB ? OFF_FB : OFF_FA) + (tile * 14 + kk) * 1024 + (((d0 >> 3) & 3) * 16 + r16) * 16 + (d0 & 7) * 2;
                    *(u32x2*)dst = pk4(acc[i][j] * sc);
                }
        }
        return;
    }
    const int gt = (b - 224) * NT + tid;
    for (int u = gt; u < 16 * 6 * 14 * 64; u += 32 * NT) {
        const int ln = u & 63, kk = (u >> 6) % 14, t = (u / 896) % 6, h = u / 5376;
        const int cl = (t & 1) * 16 + (ln & 15), d0 = kk * 32 + (ln >> 4) * 8;
        u32x4 o = {0u, 0u, 0u, 0u};
        if (cl < 28) {
            const uint16_t* src = p.wqkv + (size_t)d0 * 1344 + (t >> 1) * 448 + h * 28 + cl;
#pragma unroll
            for (int j = 0; j < 4; ++j) o[j] = (unsigned)src[(2 * j) * 1344] | ((unsigned)src[(2 * j + 1) * 1344] << 16);
        }
        *(u32x4*)(p.ws + OFF_FQ + (size_t)u * 16) = o;
    }
    for (int u = gt; u < 28 * 14 * 64; u += 32 * NT) {
        const int ln = u & 63, kk = (u >> 6) % 14, t = u / 896;
        const uint16_t* src = p.wo + (size_t)(kk * 32 + (ln >> 4) * 8) * DM + t * 16 + (ln & 15);
        u32x4 o;
#pragma unroll
        for (int j = 0; j < 4; ++j) o[j] = (unsigned)src[(2 * j) * DM] | ((unsigned)src[(2 * j + 1) * DM] << 16);
        *(u32x4*)(p.ws + OFF_FW + (size_t)u * 16) = o;
    }
    const u32x4 sent = {SENT, SENT, SENT, SENT};
#pragma unroll 1
    for (int L = 0; L < 2; ++L) {
        unsigned char* sb = p.ws + OFF_ST + (size_t)L * L_TOTAL;
        const unsigned offs[8] = {L_XN, L_XMID, L_XOUT, L_O, L_MEM + MEM_SZ, L_PM, L_PT, L_U};
        const unsigned szs[8] = {XN_SZ, XN_SZ, XN_SZ, O_SZ, MEM_SZ, PM_SZ, PT_SZ, U_SZ};
#pragma unroll
        for (int k = 0; k < 8; ++k)
            for (unsigned u = gt; u < szs[k] * ARM / 16; u += 32 * NT) *(u32x4*)(sb + offs[k] + (size_t)u * 16) = sent;
        if (gt < 16 * 7 * 8) {
            const int i = gt & 7, Q = gt >> 3, v = Q / 7, q = Q % 7;
            u32x2 m = *(const u32x2*)(p.mem0 + i * DM + Q * 4);
            m.x = nosent(m.x); m.y = nosent(m.y);
            *(u32x2*)(sb + L_MEM + v * 512 + q * 64 + i * 8) = m;
        }
    }
    if (gt == 0) *(unsigned*)(p.ws + OFF_CTRL) = 0u;
}

}

extern "C" void kernel_launch(void* const* d_in, const int* in_sizes, int n_in, void* d_out, int out_size, void* d_ws, size_t ws_size,
                              hipStream_t stream) {
    Params p;
    p.x = (const uint16_t*)d_in[0]; p.wqkv = (const uint16_t*)d_in[1]; p.wo = (const uint16_t*)d_in[2];
    p.mem0 = (const uint16_t*)d_in[4]; p.Wq = (const uint16_t*)d_in[5]; p.Wk = (const uint16_t*)d_in[6];
    p.Wv = (const uint16_t*)d_in[7]; p.Wo = (const uint16_t*)d_in[8];
    p.out = (uint16_t*)d_out; p.ws = (unsigned char*)d_ws;
    constexpr int LDS_MAIN = 140 * 1024, LDS_PRO = 64 * 528;
    (void)hipFuncSetAttribute((const void*)k_main, hipFuncAttributeMaxDynamicSharedMemorySize, LDS_MAIN);
    hipLaunchKernelGGL(k_pro, dim3(256), dim3(NT), LDS_PRO, stream, p);
    hipLaunchKernelGGL(k_main, dim3(256), dim3(NT), LDS_MAIN, stream, p);
}
```

```cpp
#include <hip/hip_runtime.h>
#include <hip/hip_bf16.h>
#include <stdint.h>

namespace {

typedef __bf16 bf16x2 __attribute__((ext_vector_type(2)));
typedef __bf16 bf16x8 __attribute__((ext_vector_type(8)));
typedef __bf16 bf16x16 __attribute__((ext_vector_type(16)));
typedef short s16x4 __attribute__((ext_vector_type(4)));
typedef float f32x4 __attribute__((ext_vector_type(4)));
typedef unsigned u32x2 __attribute__((ext_vector_type(2)));
typedef unsigned u32x4 __attribute__((ext_vector_type(4)));
typedef unsigned u32x8 __attribute__((ext_vector_type(8)));
#define LAS __attribute__((address_space(3)))
#define DI __device__ __forceinline__

constexpr int DM = 448, NSEG = 64, NT = 512;
constexpr unsigned SENT = 0xFFFFFFFFu;
constexpr unsigned SPIN_CAP = 1u << 17;

constexpr unsigned OFF_CTRL = 0;
constexpr unsigned OFF_FLG = 4096, FLG_U = 0, FLG_Y = 28u * 8 * 128, FLG_M = FLG_Y + 16u * 8 * 128, FLG_L = 49152, SZ_FLG = 2 * FLG_L;
constexpr unsigned OFF_FA = OFF_FLG + SZ_FLG, SZ_FA = 16u * 28 * 14 * 1024;
constexpr unsigned OFF_FB = OFF_FA + SZ_FA;
constexpr unsigned OFF_FQ = OFF_FB + SZ_FA, SZ_FQ = 16u * 6 * 14 * 1024;
constexpr unsigned OFF_FW = OFF_FQ + SZ_FQ, SZ_FW = 28u * 14 * 1024;
constexpr unsigned OFF_ST = OFF_FW + SZ_FW;
constexpr unsigned XI_SZ = 32768, XN_SZ = 28672, O_SZ = 32768, MEM_SZ = 8192, PM_SZ = 10240, PT_SZ = 40960, U_SZ = 458752, Y_SZ = 131072;
constexpr unsigned L_XI = 0, L_O = L_XI + 64u * XI_SZ, L_XN = L_O + 64u * O_SZ, L_XMID = L_XN + 64u * XN_SZ,
                   L_MEM = L_XMID + 64u * XN_SZ, L_PM = L_MEM + 64u * MEM_SZ, L_PT = L_PM + 64u * PM_SZ, L_ARMED = L_PT + 64u * PT_SZ,
                   L_U = L_ARMED, L_Y = L_U + 64u * U_SZ, L_TOTAL = L_Y + 64u * Y_SZ;
constexpr unsigned WS_NEED = OFF_ST + 2u * L_TOTAL;
constexpr int LDS_MAIN = 160 * 1024;

struct Params {
    const uint16_t* x;
    const uint16_t* wqkv;
    const uint16_t* wo;
    const uint16_t* mem0;
    const uint16_t* Wq;
    const uint16_t* Wk;
    const uint16_t* Wv;
    const uint16_t* Wo;
    uint16_t* out;
    unsigned char* ws;
};

typedef __amdgpu_buffer_rsrc_t rsrc_t;
DI u32x4 ld16(rsrc_t r, unsigned vo, unsigned so) { return __builtin_amdgcn_raw_buffer_load_b128(r, vo, so, 16); }
DI u32x2 ld8(rsrc_t r, unsigned vo, unsigned so) { return __builtin_amdgcn_raw_buffer_load_b64(r, vo, so, 16); }
DI unsigned ld4(rsrc_t r, unsigned vo, unsigned so) { return __builtin_amdgcn_raw_buffer_load_b32(r, vo, so, 16); }
DI void st16(rsrc_t r, unsigned vo, unsigned so, u32x4 v) { __builtin_amdgcn_raw_buffer_store_b128(v, r, vo, so, 16); }
DI void st8(rsrc_t r, unsigned vo, unsigned so, u32x2 v) { __builtin_amdgcn_raw_buffer_store_b64(v, r, vo, so, 16); }
DI void st4(rsrc_t r, unsigned vo, unsigned so, unsigned v) { __builtin_amdgcn_raw_buffer_store_b32(v, r, vo, so, 16); }
DI bool ok4(u32x4 v) { return (v.x != SENT) & (v.y != SENT) & (v.z != SENT) & (v.w != SENT); }
DI bool ok2(u32x2 v) { return (v.x != SENT) & (v.y != SENT); }
DI void drain() { asm volatile("s_waitcnt vmcnt(0)" ::: "memory"); }
DI unsigned uni(unsigned v) { return (unsigned)__builtin_amdgcn_readfirstlane((int)v); }

DI bool spin_ok(unsigned& spins, rsrc_t ws) {
    asm volatile("" ::: "memory");
    ++spins;
    if ((spins & 63u) == 0u) {
        if (spins > SPIN_CAP) { st4(ws, 0, OFF_CTRL, 1u); return false; }
        if (uni(ld4(ws, 0, OFF_CTRL)) != 0u) return false;
    }
    return true;
}
DI bool probe(rsrc_t ws, unsigned so, unsigned& spins) {
    for (;;) {
        __builtin_amdgcn_s_sleep(4);
        if (uni(ld4(ws, 0, so)) != SENT) return true;
        if (!spin_ok(spins, ws)) return false;
    }
}
DI bool retry(rsrc_t ws, unsigned so, unsigned& spins) {
    if (spins == 0u) { spins = 1u; return probe(ws, so, spins); }
    return spin_ok(spins, ws);
}
DI bool wait_flag(rsrc_t ws, unsigned vo, unsigned need, unsigned& have) {
    for (unsigned spins = 0; have < need;) {
        const unsigned v = ld4(ws, vo, OFF_FLG);
        const unsigned a = __builtin_amdgcn_readlane(v, 0), b = __builtin_amdgcn_readlane(v, 1), c = __builtin_amdgcn_readlane(v, 2);
        have = min(a, min(b, c));
        if (have >= need) break;
        __builtin_amdgcn_s_sleep(2);
        if (!spin_ok(spins, ws)) return false;
    }
    asm volatile("" ::: "memory");
    return true;
}
DI void post_flag(rsrc_t ws, unsigned so, unsigned v, int l) { if (l == 0) st4(ws, 0, OFF_FLG + so, v); }

DI unsigned pk2(float a, float b) { bf16x2 v = {(__bf16)a, (__bf16)b}; return __builtin_bit_cast(unsigned, v); }
DI unsigned nosent(unsigned x) { return x < 0xFFFFFFFEu ? x : 0xFFFFFFFEu; }
DI u32x2 pk4(f32x4 v) { return (u32x2){pk2(v[0], v[1]), pk2(v[2], v[3])}; }
DI u32x2 pk4s(f32x4 v) { return (u32x2){nosent(pk2(v[0], v[1])), nosent(pk2(v[2], v[3]))}; }
DI float bflo(unsigned x) { return __builtin_bit_cast(float, x << 16); }
DI float bfhi(unsigned x) { return __builtin_bit_cast(float, x & 0xFFFF0000u); }
DI float bfr(float x) { return (float)(__bf16)x; }
DI bf16x8 as8(u32x4 v) { return __builtin_bit_cast(bf16x8, v); }
DI bf16x8 cat8(u32x2 a, u32x2 b) { return as8((u32x4){a.x, a.y, b.x, b.y}); }
DI bf16x16 cat16(u32x4 a, u32x4 b) { return __builtin_bit_cast(bf16x16, (u32x8){a.x, a.y, a.z, a.w, b.x, b.y, b.z, b.w}); }
DI f32x4 mfma(bf16x8 a, bf16x8 b, f32x4 c) { return __builtin_amdgcn_mfma_f32_16x16x32_bf16(a, b, c, 0, 0, 0); }
DI f32x4 mfma16(u32x2 a, u32x2 b, f32x4 c) {
    return mfma(as8((u32x4){a.x, a.y, 0u, 0u}), as8((u32x4){b.x, b.y, 0u, 0u}), c);
}
DI f32x4 smfma(bf16x8 a, bf16x16 b, f32x4 c, int idx) { return __builtin_amdgcn_smfmac_f32_16x16x64_bf16(a, b, c, idx, 0, 0); }
template <int CTRL> DI float dpp(float v) {
    return __builtin_bit_cast(float, __builtin_amdgcn_update_dpp(0, __builtin_bit_cast(int, v), CTRL, 0xf, 0xf, true));
}
DI float row16_max(float v) {
    v = fmaxf(v, dpp<0x128>(v)); v = fmaxf(v, dpp<0x124>(v)); v = fmaxf(v, dpp<0x122>(v)); v = fmaxf(v, dpp<0x121>(v)); return v;
}
DI float row16_sum(float v) {
    v += dpp<0x128>(v); v += dpp<0x124>(v); v += dpp<0x122>(v); v += dpp<0x121>(v); return v;
}
DI float fbits(unsigned v) { return __builtin_bit_cast(float, v); }
DI unsigned ubits(float v) { return __builtin_bit_cast(unsigned, v); }
DI float half_add(float a, float b) {
    const auto q = __builtin_amdgcn_permlane32_swap(ubits(a), ubits(b), false, false);
    return fbits(q[0]) + fbits(q[1]);
}
DI float xg_max(float x) {
    auto q = __builtin_amdgcn_permlane32_swap(ubits(x), ubits(x), false, false);
    x = fmaxf(fbits(q[0]), fbits(q[1]));
    q = __builtin_amdgcn_permlane16_swap(ubits(x), ubits(x), false, false);
    return fmaxf(fbits(q[0]), fbits(q[1]));
}
DI float xg_sum(float x) {
    auto q = __builtin_amdgcn_permlane32_swap(ubits(x), ubits(x), false, false);
    x = fbits(q[0]) + fbits(q[1]);
    q = __builtin_amdgcn_permlane16_swap(ubits(x), ubits(x), false, false);
    return fbits(q[0]) + fbits(q[1]);
}
DI u32x4 lds16(const unsigned char* lds, unsigned off) { return *(const u32x4*)(lds + off); }
DI u32x2 lds8(const unsigned char* lds, unsigned off) { return *(const u32x2*)(lds + off); }
DI f32x4 ldsf4(const unsigned char* lds, unsigned off) { return *(const f32x4*)(lds + off); }
DI void sts16(unsigned char* lds, unsigned off, u32x4 v) { *(u32x4*)(lds + off) = v; }
DI void sts8(unsigned char* lds, unsigned off, u32x2 v) { *(u32x2*)(lds + off) = v; }
DI void sts4(unsigned char* lds, unsigned off, unsigned v) { *(unsigned*)(lds + off) = v; }
DI void stsf4(unsigned char* lds, unsigned off, f32x4 v) { *(f32x4*)(lds + off) = v; }
DI u32x2 lds_tr(unsigned char* lds, unsigned off) {
    return __builtin_bit_cast(u32x2, __builtin_amdgcn_ds_read_tr16_b64_v4i16((LAS s16x4*)(lds + off)));
}
constexpr float LOG2E = 1.4426950408889634f;
#define ZERO4 ((f32x4){0.f, 0.f, 0.f, 0.f})

#define TR(ev) do {} while (0)
#define TW(ev) do {} while (0)
struct XnRegs { u32x4 v[4]; };
DI void xn_issue(rsrc_t ws, unsigned src, int tid, XnRegs& x) {
    x.v[0] = ld16(ws, tid * 16u, src); x.v[1] = ld16(ws, tid * 16u, src + 8192u); x.v[2] = ld16(ws, tid * 16u, src + 16384u);
    x.v[3] = ld16(ws, (tid & 255) * 16u, src + 24576u);
}
DI bool xn_ok(const XnRegs& x) { return __all(ok4(x.v[0]) & ok4(x.v[1]) & ok4(x.v[2]) & ok4(x.v[3])); }
DI void xn_stage(const XnRegs& x, unsigned char* lds, unsigned xn_off, int tid) {
    sts16(lds, xn_off + tid * 16u, x.v[0]); sts16(lds, xn_off + tid * 16u + 8192u, x.v[1]); sts16(lds, xn_off + tid * 16u + 16384u, x.v[2]);
    if (tid < 256) sts16(lds, xn_off + (1536u + tid) * 16u, x.v[3]);
}
DI bool fetch_xn(rsrc_t ws, unsigned src, unsigned char* lds, unsigned xn_off, int tid, int w) {
    XnRegs x;
    for (unsigned spins = 0;;) {
        xn_issue(ws, src, tid, x);
        if (xn_ok(x)) break;
        if (!retry(ws, src + (unsigned)w * 1024u, spins)) return false;
    }
    xn_stage(x, lds, xn_off, tid);
    return true;
}
template <bool WS_, bool WK_, bool HOT>
DI bool fetch_mem(rsrc_t ws, unsigned src, unsigned char* lds, unsigned ms_off, unsigned mk_off, int tid, int w) {
    const int v = tid >> 5, q0 = (tid >> 2) & 7, rp = tid & 3;
    const int q = q0 == 7 ? 6 : q0;
    const unsigned off = (unsigned)(v * 512 + q * 64 + rp * 16);
    u32x4 m;
    for (unsigned spins = 0;;) {
        m = ld16(ws, off, src);
        if (__all(ok4(m))) break;
        if (HOT) { if (!spin_ok(spins, ws)) return false; }
        else if (!retry(ws, src + (unsigned)w * 1024u, spins)) return false;
    }
    if (q0 != 7) {
        const int Q = 7 * v + q;
        if (WK_) sts16(lds, mk_off + (unsigned)(Q * 64 + rp * 16), m);
        if (WS_) {
            const unsigned base = ms_off + (unsigned)((Q >> 4) * 1024 + ((Q >> 2) & 3) * 256 + (Q & 3) * 4 + rp * 32);
            sts4(lds, base, m.x); sts4(lds, base + 128, m.y); sts4(lds, base + 16, m.z); sts4(lds, base + 144, m.w);
        }
    }
    return true;
}
#define PRE_MS_MEM 2000
#define PRE_V_PM 2000
#define PRE_V_MEM 1200
#define GAP 5
#define XN_DELAY 24
DI unsigned now() { return (unsigned)__builtin_amdgcn_s_memtime(); }
DI void sleep_until(unsigned t0, unsigned pre) { while (now() - t0 < pre) __builtin_amdgcn_s_sleep(1); }
#define HOT_GAP 0
#define LAZY_GAP 4
DI unsigned mx4(u32x4 v) { return max(max(v.x, v.y), max(v.z, v.w)); }
DI bool none_sent(unsigned t) { return __builtin_amdgcn_ballot_w64(t == SENT) == 0ull; }
template <bool WS_, bool WK_>
DI void mem_stage(u32x4 m, unsigned char* lds, unsigned ms_off, unsigned mk_off, int tid) {
    const int v = tid >> 5, q = (tid >> 2) & 7, rp = tid & 3;
    if (q != 7) {
        const int Q = 7 * v + q;
        if (WK_) sts16(lds, mk_off + (unsigned)(Q * 64 + rp * 16), m);
        if (WS_) {
            const unsigned base = ms_off + (unsigned)((Q >> 4) * 1024 + ((Q >> 2) & 3) * 256 + (Q & 3) * 4 + rp * 32);
            sts4(lds, base, m.x); sts4(lds, base + 128, m.y); sts4(lds, base + 16, m.z); sts4(lds, base + 144, m.w);
        }
    }
}
DI unsigned mem_unit(int tid) { const int q0 = (tid >> 2) & 7; return (unsigned)((tid >> 5) * 512 + (q0 == 7 ? 6 : q0) * 64 + (tid & 3) * 16); }
DI u32x2 xn_key(const unsigned char* lds, unsigned xn_off, int nt, int mt, int g, int r16) {
    return lds8(lds, xn_off + (unsigned)(((nt >> 1) * 2 + mt) * 1024 + ((2 * (nt & 1) + (g >> 1)) * 16 + r16) * 16 + (g & 1) * 8));
}
DI u32x2 mem_key(const unsigned char* lds, unsigned mk_off, int nt, int g, int r16) {
    return lds8(lds, mk_off + (unsigned)(((4 * nt + g) * 8 + (r16 & 7)) * 8));
}

DI void role_ms(const Params& p, rsrc_t ws, int L, int h, unsigned char* lds) {
    constexpr unsigned MS_OFF = 0, MK_OFF = 7168, SP_OFF = 14336, TT_OFF = 26624, XN_OFF = 43008, W_OFF = 71680;
    const int tid = threadIdx.x, w = __builtin_amdgcn_readfirstlane(tid >> 6), l = tid & 63, g = l >> 4, r16 = l & 15;
    const unsigned SB = OFF_ST + (unsigned)L * L_TOTAL;
    const u32x4* fa = (const u32x4*)(p.ws + OFF_FA + (size_t)h * 28 * 14 * 1024);
    bf16x16 wr[3][7];
#pragma unroll
    for (int t = 0; t < 3; ++t)
#pragma unroll
        for (int ks = 0; ks < 7; ++ks)
            wr[t][ks] = cat16(fa[((3 * w + t) * 14 + 2 * ks) * 64 + l], fa[((3 * w + t) * 14 + 2 * ks + 1) * 64 + l]);
#pragma unroll
    for (int k = 0; k < 7; ++k) sts16(lds, W_OFF + (tid + 512 * k) * 16u, fa[24 * 14 * 64 + tid + 512 * k]);
    const int sidx = (r16 & 8) ? 0xEEEE : 0x4444;
    const int nt3 = 24 + (w & 3);
    const unsigned ttb = TT_OFF + (unsigned)w * 2048u;
    const unsigned xk0 = w < 4 ? 0u : 4u;
    const unsigned xm = MS_OFF + xk0 * 1024u + l * 16u, xw = W_OFF + ((unsigned)(w & 3) * 14u + 2u * xk0) * 1024u + l * 16u;

    const unsigned mu = mem_unit(tid);
    unsigned t0 = now() - PRE_MS_MEM;
    const unsigned sp0 = SP_OFF + (unsigned)(l < 32 ? 32 + l : (l & 7)) * 16u, sp1 = SP_OFF + (unsigned)(l < 32 ? 64 + l : 8 + (l & 7)) * 16u;
    for (int s = 0; s < NSEG - 1; ++s) {
        TR(0); TW(0);
        if (w != 0) __builtin_amdgcn_s_sleep(XN_DELAY);
        if (!fetch_xn(ws, SB + L_XN + (unsigned)s * XN_SZ, lds, XN_OFF, tid, w)) return;
        __syncthreads();
        TR(1); TW(1);
        {
            const unsigned src = SB + L_MEM + (unsigned)s * MEM_SZ;
            sleep_until(t0, PRE_MS_MEM);
            u32x4 m;
            for (unsigned spins = 0;;) {
                m = ld16(ws, mu, src);
                if (none_sent(mx4(m))) break;
                if (HOT_GAP) __builtin_amdgcn_s_sleep(HOT_GAP);
                if (!spin_ok(spins, ws)) return;
            }
            mem_stage<true, true>(m, lds, MS_OFF, MK_OFF, tid);
        }
        TR(2); TW(2);
        __syncthreads();
        TR(3); TW(3);
        f32x4 acc[4];
#pragma unroll
        for (int t = 0; t < 4; ++t) acc[t] = ZERO4;
#pragma unroll
        for (int ks = 0; ks < 7; ++ks) {
            const bf16x8 ma = as8(lds16(lds, MS_OFF + ks * 1024u + l * 16u));
#pragma unroll
            for (int t = 0; t < 3; ++t) acc[t] = smfma(ma, wr[t][ks], acc[t], sidx);
            if (ks < 3) {
                const unsigned o = xw + ks * 2048u;
                acc[3] = smfma(as8(lds16(lds, xm + ks * 1024u)), cat16(lds16(lds, o), lds16(lds, o + 1024)), acc[3], sidx);
            }
        }
        if (w < 4) acc[3] = smfma(as8(lds16(lds, xm + 3072u)), cat16(lds16(lds, xw + 6144u), lds16(lds, xw + 7168u)), acc[3], sidx);
        TR(4); TW(4);
        u32x2 tf[4];
#pragma unroll
        for (int t = 0; t < 4; ++t) sts8(lds, ttb + t * 512u + r16 * 32u + g * 8u, pk4(acc[t]));
#pragma unroll
        for (int t = 0; t < 4; ++t) tf[t] = lds_tr(lds, ttb + t * 512u + (4 * g + (r16 >> 2)) * 32u + (r16 & 3) * 8u);
        TW(5);
        f32x4 sa[3];
#pragma unroll
        for (int k = 0; k < 3; ++k) sa[k] = ZERO4;
#pragma unroll
        for (int pr = 0; pr < 2; ++pr) {
            const bf16x8 tb = cat8(tf[2 * pr], tf[2 * pr + 1]);
            const int na = 3 * w + 2 * pr, nb = pr == 0 ? 3 * w + 1 : nt3;
            sa[0] = mfma(cat8(mem_key(lds, MK_OFF, na, g, r16), mem_key(lds, MK_OFF, nb, g, r16)), tb, sa[0]);
            sa[1] = mfma(cat8(xn_key(lds, XN_OFF, na, 0, g, r16), xn_key(lds, XN_OFF, nb, 0, g, r16)), tb, sa[1]);
            sa[2] = mfma(cat8(xn_key(lds, XN_OFF, na, 1, g, r16), xn_key(lds, XN_OFF, nb, 1, g, r16)), tb, sa[2]);
        }
#pragma unroll
        for (int k = 0; k < 3; ++k) {
#pragma unroll
            for (int r = 0; r < 4; ++r) sa[k][r] += dpp<0x128>(sa[k][r]);
            if (r16 < 8) stsf4(lds, SP_OFF + (unsigned)(((w * 3 + k) * 32 + g * 8 + r16) * 16), sa[k]);
        }
        TW(6);
        __syncthreads();
        TR(5); TW(7);
        if (w != 0) t0 = now();
        if (w == 0) {
            f32x4 a, b;
            {
                f32x4 s0[4], s1[4];
#pragma unroll
                for (int ww = 0; ww < 4; ++ww) { s0[ww] = ldsf4(lds, sp0 + ww * 1536u); s1[ww] = ldsf4(lds, sp1 + ww * 1536u); }
                f32x4 u0[2], u1[2];
#pragma unroll
                for (int ww = 0; ww < 2; ++ww) { u0[ww] = ldsf4(lds, sp0 + (4 + ww) * 1536u); u1[ww] = ldsf4(lds, sp1 + (4 + ww) * 1536u); }
                a = (s0[0] + s0[1]) + (s0[2] + s0[3]); b = (s1[0] + s1[1]) + (s1[2] + s1[3]);
#pragma unroll
                for (int ww = 0; ww < 2; ++ww) { s0[ww] = ldsf4(lds, sp0 + (6 + ww) * 1536u); s1[ww] = ldsf4(lds, sp1 + (6 + ww) * 1536u); }
                a += (u0[0] + u0[1]) + (s0[0] + s0[1]); b += (u1[0] + u1[1]) + (s1[0] + s1[1]);
            }
            TW(8);
            float mx = fmaxf(fmaxf(fmaxf(a[0], a[1]), fmaxf(a[2], a[3])), fmaxf(fmaxf(b[0], b[1]), fmaxf(b[2], b[3])));
            mx = xg_max(fmaxf(mx, dpp<0x128>(mx))) * LOG2E;
            f32x4 ea, eb;
#pragma unroll
            for (int r = 0; r < 4; ++r) {
                ea[r] = __builtin_amdgcn_exp2f(__builtin_fmaf(a[r], LOG2E, -mx)); eb[r] = __builtin_amdgcn_exp2f(__builtin_fmaf(b[r], LOG2E, -mx));
            }
            const f32x4 es = ea + eb;
            float part = l < 40 ? (es[0] + es[1]) + (es[2] + es[3]) : 0.f;
            part += dpp<0x128>(part);
            const float inv = __builtin_amdgcn_rcpf(xg_sum(part));
            const u32x2 pa = pk4s(ea * inv), pb = pk4s(eb * inv);
            if (l < 40) st16(ws, (unsigned)l * 16u, SB + L_PM + (unsigned)s * PM_SZ + (unsigned)h * 640u, (u32x4){pa.x, pa.y, pb.x, pb.y});
            TR(6); TW(9);
            t0 = now() - 500u;
        }
    }
}

DI void role_v(const Params& p, rsrc_t ws, int L, int v, unsigned char* lds) {
    constexpr unsigned MS_OFF = 0, Y_OFF = 7168, OP_OFF = 15360, W2_OFF = 23552, W3_OFF = W2_OFF + 65536;
    const int tid = threadIdx.x, w = __builtin_amdgcn_readfirstlane(tid >> 6), l = tid & 63, g = l >> 4, r16 = l & 15;
    const unsigned SB = OFF_ST + (unsigned)L * L_TOTAL;
    const u32x4* fb = (const u32x4*)(p.ws + OFF_FB);
    auto wunit = [&](int tile, int kk, int gg, int n16) -> u32x4 {
        const int wc = tile * 16 + n16, hh = wc / 28, c = 28 * v + wc % 28;
        return fb[(((c >> 4) * 16 + hh) * 14 + kk) * 64 + gg * 16 + (c & 15)];
    };
#pragma unroll
    for (int k = 0; k < 8; ++k) sts16(lds, W2_OFF + (unsigned)((w * 8 + k) * 1024 + l * 16), wunit(16 + w, 6 + k, g, r16));
#pragma unroll
    for (int k = 0; k < 7; ++k) {
        const int u = tid + 512 * k;
        sts16(lds, W3_OFF + u * 16u, wunit(24 + u / 896, (u / 64) % 14, (u >> 4) & 3, u & 15));
    }
    bf16x16 wr[2][7], wr2[3];
#pragma unroll
    for (int t = 0; t < 2; ++t)
#pragma unroll
        for (int ks = 0; ks < 7; ++ks) wr[t][ks] = cat16(wunit(2 * w + t, 2 * ks, g, r16), wunit(2 * w + t, 2 * ks + 1, g, r16));
#pragma unroll
    for (int ks = 0; ks < 3; ++ks) wr2[ks] = cat16(wunit(16 + w, 2 * ks, g, r16), wunit(16 + w, 2 * ks + 1, g, r16));
    const int sidx = (r16 & 8) ? 0xEEEE : 0x4444;
    const int ha = 2 * w;
    unsigned uoff[2]; unsigned yoff[2];
#pragma unroll
    for (int t = 0; t < 2; ++t) {
        int cl = 16 * t + r16; if (cl > 27) cl = 27;
        const int c = 28 * v + cl;
        uoff[t] = (unsigned)(((c >> 4) * 16 * 64 + g * 16 + (c & 15)) * 16 + ha * 1024);
        yoff[t] = Y_OFF + (unsigned)(((ha + (g >> 1)) * 28 + cl) * 16 + (g & 1) * 8);
    }
    const unsigned ptok = (unsigned)(ha * 640 + (g * 8 + (r16 & 7)) * 16);
    const unsigned pmem = (unsigned)((ha + (g >> 1)) * 640 + 512 + (r16 & 7) * 16 + (g & 1) * 8);
    const unsigned ys0 = Y_OFF + (unsigned)(((2 * w + (g >> 1)) * 16 + r16) * 16 + (g & 1) * 8);
    const unsigned ys1 = Y_OFF + (unsigned)((((g >> 1) ? 24 + w : 16 + w) * 16 + r16) * 16 + (g & 1) * 8);
    const unsigned w2 = W2_OFF + (unsigned)(w * 8192 + l * 16), w3 = W3_OFF + (unsigned)((w & 3) * 14336 + l * 16);
    const int ct0 = (28 * v) >> 4, ct1 = (28 * v + 27) >> 4;
    const unsigned fU = (unsigned)L * FLG_L + FLG_U + (unsigned)((min(ct0 + min(l, 2), ct1) * 8 + w) * 128);
    const unsigned fY = (unsigned)L * FLG_L + FLG_Y + (unsigned)((v * 8 + w) * 128);
    unsigned haveU = 0;
    const unsigned mu = mem_unit(tid);
    unsigned t0 = now() - PRE_V_MEM;

    for (int s = 0; s < NSEG; ++s) {
        TR(0); TW(0);
        {
            const unsigned src = SB + L_MEM + (unsigned)s * MEM_SZ;
            sleep_until(t0, PRE_V_MEM);
            TR(1); TW(1);
            u32x4 m;
            for (unsigned spins = 0;;) {
                m = ld16(ws, mu, src);
                if (none_sent(mx4(m))) break;
                __builtin_amdgcn_s_sleep(LAZY_GAP);
                if (!spin_ok(spins, ws)) return;
            }
            mem_stage<true, false>(m, lds, MS_OFF, 0, tid);
        }
        TR(2); TW(2);
        __syncthreads();
        TW(3);
        t0 = now();
        if (v == 0 && w == 7) post_flag(ws, (unsigned)L * FLG_L + FLG_M, (unsigned)s + 1u, l);
        u32x4 uf[2][2];
        {
            const int su = s < NSEG - 1 ? s : NSEG - 2;
            if (!wait_flag(ws, fU, (unsigned)su + 1u, haveU)) return;
            const unsigned ub = SB + L_U + (unsigned)su * U_SZ;
#pragma unroll
            for (int t = 0; t < 2; ++t) { uf[0][t] = ld16(ws, uoff[t], ub); uf[1][t] = ld16(ws, uoff[t], ub + 1024u); }
        }
        asm volatile("" ::: "memory");
        f32x4 acc[4];
#pragma unroll
        for (int t = 0; t < 4; ++t) acc[t] = ZERO4;
#pragma unroll
        for (int ks = 0; ks < 7; ++ks) {
            const bf16x8 ma = as8(lds16(lds, MS_OFF + ks * 1024u + l * 16u));
#pragma unroll
            for (int t = 0; t < 2; ++t) acc[t] = smfma(ma, wr[t][ks], acc[t], sidx);
            if (ks < 3) acc[2] = smfma(ma, wr2[ks], acc[2], sidx);
            else acc[2] = smfma(ma, cat16(lds16(lds, w2 + (ks - 3) * 2048u), lds16(lds, w2 + (ks - 3) * 2048u + 1024u)), acc[2], sidx);
        }
        if (w < 4) {
            asm volatile("" ::: "memory");
#pragma unroll
            for (int ks = 0; ks < 7; ++ks)
                acc[3] = smfma(as8(lds16(lds, MS_OFF + ks * 1024u + l * 16u)),
                               cat16(lds16(lds, w3 + ks * 2048u), lds16(lds, w3 + ks * 2048u + 1024u)), acc[3], sidx);
        }
        TW(4);
        {
            f32x4 y0, y1;
#pragma unroll
            for (int r = 0; r < 4; ++r) { y0[r] = half_add(acc[0][r], acc[1][r]); y1[r] = half_add(acc[2][r], acc[3][r]); }
            sts8(lds, ys0, pk4(y0)); sts8(lds, ys1, pk4(y1));
        }
        __syncthreads();
        const u32x2 yf0 = lds8(lds, yoff[0]), yf1 = lds8(lds, yoff[1]);
        st16(ws, (unsigned)tid * 16u, SB + L_Y + (unsigned)s * Y_SZ + (unsigned)v * 8192u, (u32x4){yf0.x, yf0.y, yf1.x, yf1.y});
        asm volatile("" ::: "memory");
        if (s == NSEG - 1) { drain(); post_flag(ws, fY, (unsigned)NSEG, l); break; }
        u32x4 ya = {yf0.x, yf0.y, 0u, 0u}, yb = {yf1.x, yf1.y, 0u, 0u};
        TR(3); TW(5);
        {
            const unsigned pbase = SB + L_PM + (unsigned)s * PM_SZ;
            sleep_until(t0, PRE_V_PM);
            u32x4 pa, pb; u32x2 qm;
            for (unsigned spins = 0;;) {
                pa = ld16(ws, ptok, pbase); pb = ld16(ws, ptok, pbase + 640u); qm = ld8(ws, pmem, pbase);
                if (none_sent(max(max(mx4(pa), mx4(pb)), max(qm.x, qm.y)))) break;
                if (HOT_GAP) __builtin_amdgcn_s_sleep(HOT_GAP);
                if (!spin_ok(spins, ws)) return;
            }
            TR(4); TW(6);
            const u32x4 q4 = {qm.x, qm.y, 0u, 0u};
            f32x4 o0 = mfma(as8(uf[0][0]), as8(pa), ZERO4), o1 = mfma(as8(uf[0][1]), as8(pa), ZERO4);
            o0 = mfma(as8(uf[1][0]), as8(pb), o0); o1 = mfma(as8(uf[1][1]), as8(pb), o1);
            o0 = mfma(as8(ya), as8(q4), o0); o1 = mfma(as8(yb), as8(q4), o1);
            if (r16 < 8) {
                stsf4(lds, OP_OFF + (unsigned)((w * 64 + g * 8 + r16) * 16), o0);
                stsf4(lds, OP_OFF + (unsigned)((w * 64 + 32 + g * 8 + r16) * 16), o1);
            }
        }
        TW(7);
        __syncthreads();
        TR(5); TW(8);
        if (w != 0) t0 = now();
        if (w == 0) {
            const int lc = l < 56 ? l : 55;
            f32x4 o[8];
#pragma unroll
            for (int ww = 0; ww < 8; ++ww) o[ww] = ldsf4(lds, OP_OFF + (unsigned)((ww * 64 + lc) * 16));
            const f32x4 r = ((o[0] + o[1]) + (o[2] + o[3])) + ((o[4] + o[5]) + (o[6] + o[7]));
            if (l < 56) st8(ws, (unsigned)l * 8u, SB + L_MEM + (unsigned)(s + 1) * MEM_SZ + (unsigned)v * 512u, pk4s(r));
            TR(6); TW(9);
            t0 = now() - 300u;
        }
        post_flag(ws, fY, (unsigned)s + 1u, l);
    }
}

DI void role_ts(const Params& p, rsrc_t ws, int L, int y, unsigned char* lds) {
    constexpr unsigned XN_OFF = 0, MK_OFF = 28672, SP_OFF = 35840, W_OFF = 76800;
    const int h = y >> 1;
    const int tid = threadIdx.x, w = __builtin_amdgcn_readfirstlane(tid >> 6), l = tid & 63, g = l >> 4, r16 = l & 15;
    const unsigned SB = OFF_ST + (unsigned)L * L_TOTAL;
    const u32x4* fa = (const u32x4*)(p.ws + OFF_FA + (size_t)h * 28 * 14 * 1024);
    bf16x8 wr[3][14];
#pragma unroll
    for (int t = 0; t < 3; ++t)
#pragma unroll
        for (int kk = 0; kk < 14; ++kk) wr[t][kk] = as8(fa[((3 * w + t) * 14 + kk) * 64 + l]);
#pragma unroll
    for (int k = 0; k < 7; ++k) sts16(lds, W_OFF + (tid + 512 * k) * 16u, fa[24 * 14 * 64 + tid + 512 * k]);
    const int nt3 = 24 + (w & 3);
    const unsigned xw = W_OFF + (unsigned)(w & 3) * 14336u + l * 16u, xx = XN_OFF + (unsigned)(w >> 2) * 1024u + l * 16u;

    unsigned haveM = 0;
    for (int s = y & 1; s < NSEG; s += 2) {
        TR(0);
        if (!fetch_xn(ws, SB + L_XN + (unsigned)s * XN_SZ, lds, XN_OFF, tid, w)) return;
        __syncthreads();
        TR(1);
        u32x2 tf[2][4];
#pragma unroll
        for (int mt = 0; mt < 2; ++mt) {
            f32x4 acc[3];
#pragma unroll
            for (int t = 0; t < 3; ++t) acc[t] = ZERO4;
#pragma unroll
            for (int kk = 0; kk < 14; ++kk) {
                const bf16x8 xb = as8(lds16(lds, XN_OFF + (unsigned)((kk * 2 + mt) * 1024 + l * 16)));
#pragma unroll
                for (int t = 0; t < 3; ++t) acc[t] = mfma(wr[t][kk], xb, acc[t]);
            }
#pragma unroll
            for (int t = 0; t < 3; ++t) tf[mt][t] = pk4(acc[t]);
        }
        {
            f32x4 acc = ZERO4;
#pragma unroll
            for (int kk = 0; kk < 14; ++kk)
                acc = mfma(as8(lds16(lds, xw + (unsigned)kk * 1024u)), as8(lds16(lds, xx + (unsigned)kk * 2048u)), acc);
            const u32x2 t3 = pk4(acc), z = {0u, 0u};
            tf[0][3] = w < 4 ? t3 : z; tf[1][3] = w < 4 ? z : t3;
        }
        f32x4 sa[5];
#pragma unroll
        for (int k = 0; k < 5; ++k) sa[k] = ZERO4;
#pragma unroll
        for (int pr = 0; pr < 2; ++pr) {
            const int na = 3 * w + 2 * pr, nb = pr == 0 ? 3 * w + 1 : nt3;
            const bf16x8 k0 = cat8(xn_key(lds, XN_OFF, na, 0, g, r16), xn_key(lds, XN_OFF, nb, 0, g, r16));
            const bf16x8 k1 = cat8(xn_key(lds, XN_OFF, na, 1, g, r16), xn_key(lds, XN_OFF, nb, 1, g, r16));
            const bf16x8 t0 = cat8(tf[0][2 * pr], tf[0][2 * pr + 1]), t1 = cat8(tf[1][2 * pr], tf[1][2 * pr + 1]);
            sa[2] = mfma(k0, t0, sa[2]); sa[3] = mfma(k0, t1, sa[3]); sa[4] = mfma(k1, t1, sa[4]);
        }
#pragma unroll
        for (int k = 2; k < 5; ++k) stsf4(lds, SP_OFF + (unsigned)(((w * 5 + k) * 64 + l) * 16), sa[k]);
        TR(2);
        if (!wait_flag(ws, (unsigned)L * FLG_L + FLG_M, (unsigned)s + 1u, haveM)) return;
        if (!fetch_mem<false, true, false>(ws, SB + L_MEM + (unsigned)s * MEM_SZ, lds, 0, MK_OFF, tid, w)) return;
        __syncthreads();
        TR(3);
#pragma unroll
        for (int pr = 0; pr < 2; ++pr) {
            const int na = 3 * w + 2 * pr, nb = pr == 0 ? 3 * w + 1 : nt3;
            const bf16x8 km = cat8(mem_key(lds, MK_OFF, na, g, r16), mem_key(lds, MK_OFF, nb, g, r16));
            sa[0] = mfma(km, cat8(tf[0][2 * pr], tf[0][2 * pr + 1]), sa[0]);
            sa[1] = mfma(km, cat8(tf[1][2 * pr], tf[1][2 * pr + 1]), sa[1]);
        }
#pragma unroll
        for (int k = 0; k < 2; ++k) stsf4(lds, SP_OFF + (unsigned)(((w * 5 + k) * 64 + l) * 16), sa[k]);
        __syncthreads();
        {
            const int row = tid >> 4, q = tid & 15, mt = row >> 4, i = row & 15;
            const int qq = q < 10 ? q : 9;
            const int kt = qq < 2 ? 0 : (qq < 6 ? 1 : 2), gq = qq < 2 ? qq : ((qq - 2) & 3);
            const bool live = (q < 10) && !(kt == 2 && mt == 0);
            const int tile = kt == 0 ? mt : (kt == 1 ? 2 + mt : 4);
            f32x4 sv = ZERO4;
#pragma unroll
            for (int ww = 0; ww < 8; ++ww) sv += ldsf4(lds, SP_OFF + (unsigned)(((ww * 5 + tile) * 64 + gq * 16 + i) * 16));
            bool lv[4];
#pragma unroll
            for (int r = 0; r < 4; ++r) {
                const int key = (kt - 1) * 16 + 4 * gq + r;
                lv[r] = live && (kt == 0 || key <= row);
                sv[r] = lv[r] ? sv[r] : -3.0e38f;
            }
            const float mx = row16_max(fmaxf(fmaxf(sv[0], sv[1]), fmaxf(sv[2], sv[3])));
            f32x4 e;
#pragma unroll
            for (int r = 0; r < 4; ++r) e[r] = lv[r] ? __builtin_amdgcn_exp2f((sv[r] - mx) * LOG2E) : 0.f;
            const float inv = __builtin_amdgcn_rcpf(row16_sum((e[0] + e[1]) + (e[2] + e[3])));
            e *= inv;
            const unsigned po = (unsigned)h * 2560u + (unsigned)mt * 1280u +
                                (kt == 0 ? 1024u + (unsigned)(gq * 16 + i) * 8u : (unsigned)(gq * 16 + i) * 16u + (unsigned)(kt - 1) * 8u);
            if (q < 10) st8(ws, po, SB + L_PT + (unsigned)s * PT_SZ, pk4s(e));
            TR(6);
        }
    }
}

DI void role_vu(const Params& p, rsrc_t ws, int L, int ct, unsigned char* lds) {
    const int tid = threadIdx.x, w = __builtin_amdgcn_readfirstlane(tid >> 6), l = tid & 63;
    const unsigned SB = OFF_ST + (unsigned)L * L_TOTAL;
    const u32x4* fb = (const u32x4*)(p.ws + OFF_FB + (size_t)ct * 16 * 14 * 1024);
    bf16x8 wr[2][14];
#pragma unroll
    for (int t = 0; t < 2; ++t)
#pragma unroll
        for (int kk = 0; kk < 14; ++kk) wr[t][kk] = as8(fb[((2 * w + t) * 14 + kk) * 64 + l]);
    const unsigned fU = (unsigned)L * FLG_L + FLG_U + (unsigned)((ct * 8 + w) * 128);
    const unsigned uo = (unsigned)((ct * 16 + 2 * w) * 1024 + l * 16);
    XnRegs x;
    xn_issue(ws, SB + L_XN, tid, x);
    int posted = 0;
    for (int s = 0; s < NSEG; ++s) {
        TR(0);
        const unsigned xo = (unsigned)(s & 1) * XN_SZ, src = SB + L_XN + (unsigned)s * XN_SZ;
        for (unsigned spins = 0; !xn_ok(x);) {
            if (posted < s) { drain(); post_flag(ws, fU, (unsigned)s, l); posted = s; }
            if (!retry(ws, src + (unsigned)w * 1024u, spins)) return;
            xn_issue(ws, src, tid, x);
        }
        xn_stage(x, lds, xo, tid);
        __syncthreads();
        TR(1);
        asm volatile("" ::: "memory");
        xn_issue(ws, s + 1 < NSEG ? src + XN_SZ : src, tid, x);
        asm volatile("" ::: "memory");
        f32x4 acc[2][2];
#pragma unroll
        for (int t = 0; t < 2; ++t) { acc[t][0] = ZERO4; acc[t][1] = ZERO4; }
#pragma unroll
        for (int kk = 0; kk < 14; ++kk)
#pragma unroll
            for (int mt = 0; mt < 2; ++mt) {
                const bf16x8 xa = as8(lds16(lds, xo + (unsigned)((kk * 2 + mt) * 1024 + l * 16)));
#pragma unroll
                for (int t = 0; t < 2; ++t) acc[t][mt] = mfma(xa, wr[t][kk], acc[t][mt]);
            }
        if (posted < s) {
            asm volatile("s_waitcnt vmcnt(4)" ::: "memory");
            post_flag(ws, fU, (unsigned)s, l); posted = s;
        }
#pragma unroll
        for (int t = 0; t < 2; ++t) {
            const u32x2 a = pk4(acc[t][0]), b = pk4(acc[t][1]);
            st16(ws, uo, SB + L_U + (unsigned)s * U_SZ + t * 1024u, (u32x4){a.x, a.y, b.x, b.y});
        }
        TR(6);
    }
    drain();
    post_flag(ws, fU, (unsigned)NSEG, l);
}

DI void role_vo(const Params& p, rsrc_t ws, int L, int v, unsigned char* lds) {
    const int tid = threadIdx.x, w = __builtin_amdgcn_readfirstlane(tid >> 6), l = tid & 63, g = l >> 4, r16 = l & 15;
    const unsigned SB = OFF_ST + (unsigned)L * L_TOTAL;
    const int ha = 2 * w;
    unsigned uoff[2];
#pragma unroll
    for (int t = 0; t < 2; ++t) {
        int cl = 16 * t + r16; if (cl > 27) cl = 27;
        const int c = 28 * v + cl;
        uoff[t] = (unsigned)(((c >> 4) * 16 * 64 + g * 16 + (c & 15)) * 16 + ha * 1024);
    }
    const unsigned ptok = (unsigned)(ha * 2560 + l * 16), pmem = (unsigned)((ha + (g >> 1)) * 2560 + 1024 + ((g & 1) * 16 + r16) * 8);
    const int ct0 = (28 * v) >> 4, ct1 = (28 * v + 27) >> 4;
    const unsigned fU = (unsigned)L * FLG_L + FLG_U + (unsigned)((min(ct0 + min(l, 2), ct1) * 8 + w) * 128);
    const unsigned fY = (unsigned)L * FLG_L + FLG_Y + (unsigned)((v * 8 + w) * 128);
    unsigned haveU = 0, haveY = 0;
    const int row = (tid >> 3) & 31, qd = tid & 7, qc = qd < 7 ? qd : 6;
    const int Q = 7 * v + qc;
    const unsigned xmo = (unsigned)((Q >> 2) * 1024 + row * 32 + (Q & 3) * 8);
    const unsigned rdo = (unsigned)((((qc >> 2) * 2 + (row >> 4)) * 64 + (qc & 3) * 16 + (row & 15)) * 16);
    for (int s = 0; s < NSEG; ++s) {
        TR(0);
        if (!wait_flag(ws, fU, (unsigned)s + 1u, haveU)) return;
        u32x4 uf[2][2];
        {
            const unsigned ub = SB + L_U + (unsigned)s * U_SZ;
#pragma unroll
            for (int t = 0; t < 2; ++t) { uf[0][t] = ld16(ws, uoff[t], ub); uf[1][t] = ld16(ws, uoff[t], ub + 1024u); }
        }
        if (!wait_flag(ws, fY, (unsigned)s + 1u, haveY)) return;
        const u32x4 yf = ld16(ws, (unsigned)tid * 16u, SB + L_Y + (unsigned)s * Y_SZ + (unsigned)v * 8192u);
        TR(1);
        u32x4 pa[2], pb[2]; u32x2 qm[2], xr;
        {
            const unsigned pbase = SB + L_PT + (unsigned)s * PT_SZ;
            for (unsigned spins = 0;;) {
#pragma unroll
                for (int mt = 0; mt < 2; ++mt) {
                    pa[mt] = ld16(ws, ptok, pbase + mt * 1280u); pb[mt] = ld16(ws, ptok, pbase + 2560u + mt * 1280u);
                    qm[mt] = ld8(ws, pmem, pbase + mt * 1280u);
                }
                xr = ld8(ws, xmo, SB + L_XMID + (unsigned)s * XN_SZ);
                bool ok = ok2(xr);
#pragma unroll
                for (int mt = 0; mt < 2; ++mt) ok = ok & ok4(pa[mt]) & ok4(pb[mt]) & ok2(qm[mt]);
                if (__all(ok)) break;
                if (!retry(ws, pbase + (unsigned)ha * 2560u, spins)) return;
            }
        }
        TR(2);
        const unsigned ob = (unsigned)(s & 1) * 32768u;
#pragma unroll
        for (int t = 0; t < 2; ++t)
#pragma unroll
            for (int mt = 0; mt < 2; ++mt) {
                f32x4 o = ZERO4;
                o = mfma(as8(uf[0][t]), as8(pa[mt]), o);
                o = mfma(as8(uf[1][t]), as8(pb[mt]), o);
                o = mfma16(t ? (u32x2){yf.z, yf.w} : (u32x2){yf.x, yf.y}, qm[mt], o);
                stsf4(lds, ob + (unsigned)((((w * 2 + t) * 2 + mt) * 64 + l) * 16), o);
            }
        __syncthreads();
        if (w < 4) {
            f32x4 o = ZERO4;
#pragma unroll
            for (int ww = 0; ww < 8; ++ww) o += ldsf4(lds, ob + rdo + (unsigned)ww * 4096u);
            f32x4 r;
            r[0] = bflo(xr.x) + bfr(o[0]); r[1] = bfhi(xr.x) + bfr(o[1]); r[2] = bflo(xr.y) + bfr(o[2]); r[3] = bfhi(xr.y) + bfr(o[3]);
            if (L == 0) {
                u32x2 pv = pk4s(r);
                if (qd == 7) pv = (u32x2){0u, 0u};
                st8(ws, (unsigned)tid * 8u, OFF_ST + L_TOTAL + L_XI + (unsigned)s * XI_SZ + (unsigned)v * 2048u, pv);
            } else if (qd < 7) {
                *(u32x2*)(p.out + (size_t)(s * 32 + row) * DM + Q * 4) = pk4(r);
            }
            TR(6);
        }
    }
}

DI void role_wo(const Params& p, rsrc_t ws, int L, int y, unsigned char* lds) {
    constexpr unsigned OI_OFF = 0, ROWB = 912, RED_OFF = OI_OFF + 16 * ROWB, W_OFF = 16384;
    const int mt = y & 1;
    const int tid = threadIdx.x, w = __builtin_amdgcn_readfirstlane(tid >> 6), l = tid & 63, g = l >> 4, r16 = l & 15;
    const unsigned SB = OFF_ST + (unsigned)L * L_TOTAL;
    const u32x4* fw = (const u32x4*)(p.ws + OFF_FW);
    bf16x8 wr[3][14];
#pragma unroll
    for (int t = 0; t < 3; ++t)
#pragma unroll
        for (int kk = 0; kk < 14; ++kk) wr[t][kk] = as8(fw[((3 * w + t) * 14 + kk) * 64 + l]);
#pragma unroll
    for (int k = 0; k < 7; ++k) sts16(lds, W_OFF + (tid + 512 * k) * 16u, fw[24 * 14 * 64 + tid + 512 * k]);
    const int ntile = w < 4 ? 4 : 3;
    float* red = (float*)(lds + RED_OFF);
    unsigned xio[4];
#pragma unroll
    for (int t = 0; t < 4; ++t) {
        const int nt = t < 3 ? 3 * w + t : 24 + (w & 3), Q = 4 * nt + g;
        xio[t] = (unsigned)((Q / 7) * 2048 + (mt * 16 + r16) * 64 + (Q % 7) * 8);
    }
    const unsigned oo = (unsigned)(w * 2048 + mt * 1024 + l * 16);
    for (int s = (y >> 1) & 1; s < NSEG; s += 2) {
        TR(0);
        u32x4 ov[2]; u32x2 xr[4];
        {
            const unsigned ob = SB + L_O + (unsigned)s * O_SZ, xb = SB + L_XI + (unsigned)s * XI_SZ;
            for (unsigned spins = 0;;) {
                ov[0] = ld16(ws, oo, ob); ov[1] = ld16(ws, oo, ob + 16384u);
                bool ok = ok4(ov[0]) & ok4(ov[1]);
#pragma unroll
                for (int t = 0; t < 4; ++t) { xr[t] = ld8(ws, xio[t], xb); ok = ok & ok2(xr[t]); }
                if (__all(ok)) break;
                if (!retry(ws, ob + (unsigned)(w * 2048 + mt * 1024), spins)) return;
            }
        }
        TR(1);
#pragma unroll
        for (int k = 0; k < 2; ++k) {
            const int hh = w + 8 * k, row = l >> 2, u4 = l & 3;
            const unsigned d = OI_OFF + (unsigned)(row * ROWB + hh * 56 + u4 * 16);
            sts8(lds, d, (u32x2){ov[k].x, ov[k].y});
            if (u4 != 3) sts8(lds, d + 8, (u32x2){ov[k].z, ov[k].w});
        }
        __syncthreads();
        f32x4 acc[4];
#pragma unroll
        for (int t = 0; t < 4; ++t) acc[t] = ZERO4;
#pragma unroll
        for (int kk = 0; kk < 14; ++kk) {
            const bf16x8 ob = as8(lds16(lds, OI_OFF + (unsigned)(r16 * ROWB + kk * 64 + g * 16)));
#pragma unroll
            for (int t = 0; t < 3; ++t) acc[t] = mfma(wr[t][kk], ob, acc[t]);
        }
        if (w < 4) {
            asm volatile("" ::: "memory");
#pragma unroll
            for (int kk = 0; kk < 14; ++kk)
                acc[3] = mfma(as8(lds16(lds, W_OFF + (unsigned)((w * 14 + kk) * 1024 + l * 16))),
                              as8(lds16(lds, OI_OFF + (unsigned)(r16 * ROWB + kk * 64 + g * 16))), acc[3]);
        }
        TR(2);
        float xm[4][4]; float sum = 0.f;
#pragma unroll
        for (int t = 0; t < 4; ++t) {
            xm[t][0] = bfr(bflo(xr[t].x) + bfr(acc[t][0])); xm[t][1] = bfr(bfhi(xr[t].x) + bfr(acc[t][1]));
            xm[t][2] = bfr(bflo(xr[t].y) + bfr(acc[t][2])); xm[t][3] = bfr(bfhi(xr[t].y) + bfr(acc[t][3]));
            if (t < ntile) sum += (xm[t][0] + xm[t][1]) + (xm[t][2] + xm[t][3]);
        }
        sum = xg_sum(sum);
        if (l < 16) red[w * 16 + l] = sum;
#pragma unroll
        for (int t = 0; t < 4; ++t)
            if (t < ntile) {
                const int nt = t < 3 ? 3 * w + t : 24 + w;
                st8(ws, (unsigned)((mt * 16 + r16) * 32 + g * 8), SB + L_XMID + (unsigned)s * XN_SZ + (unsigned)nt * 1024u,
                    pk4s((f32x4){xm[t][0], xm[t][1], xm[t][2], xm[t][3]}));
            }
        __syncthreads();
        float mean = 0.f;
#pragma unroll
        for (int ww = 0; ww < 8; ++ww) mean += red[ww * 16 + r16];
        mean *= (1.0f / DM);
        float sq = 0.f;
#pragma unroll
        for (int t = 0; t < 4; ++t)
            if (t < ntile) {
#pragma unroll
                for (int r = 0; r < 4; ++r) { const float dlt = xm[t][r] - mean; sq += dlt * dlt; }
            }
        sq = xg_sum(sq);
        if (l < 16) red[128 + w * 16 + l] = sq;
        __syncthreads();
        float var = 0.f;
#pragma unroll
        for (int ww = 0; ww < 8; ++ww) var += red[128 + ww * 16 + r16];
        const float rstd = 1.0f / sqrtf(var * (1.0f / DM) + 1e-5f);
#pragma unroll
        for (int t = 0; t < 4; ++t)
            if (t < ntile) {
                const int nt = t < 3 ? 3 * w + t : 24 + w;
                const f32x4 xv = {xm[t][0], xm[t][1], xm[t][2], xm[t][3]};
                const f32x4 nv = (xv - mean) * rstd;
                st8(ws, (unsigned)(((g >> 1) * 16 + r16) * 16 + (g & 1) * 8),
                    SB + L_XN + (unsigned)s * XN_SZ + (unsigned)(((nt >> 1) * 2 + mt) * 1024 + (nt & 1) * 512), pk4s(nv));
            }
        TR(6);
    }
}

DI void role_at(const Params& p, rsrc_t ws, int L, int h, unsigned char* lds) {
    constexpr unsigned MGW = 2048 + 128;
    constexpr unsigned RB = 80, VRB = 1040, MG_OFF = 0, Q_OFF = 8 * MGW, XA_OFF = Q_OFF + 32 * RB, K_OFF = XA_OFF + 28672,
                       V_OFF = K_OFF + 512 * RB;
    const int tid = threadIdx.x, w = __builtin_amdgcn_readfirstlane(tid >> 6), l = tid & 63, g = l >> 4, r16 = l & 15;
    const unsigned SB = OFF_ST + (unsigned)L * L_TOTAL;
    const u32x4* fq = (const u32x4*)(p.ws + OFF_FQ + (size_t)h * 6 * 14 * 1024);
    const int wt = w < 4 ? w : 4 + (w & 1);
    bf16x8 wr[14];
#pragma unroll
    for (int kk = 0; kk < 14; ++kk) wr[kk] = as8(fq[(wt * 14 + kk) * 64 + l]);
    const int row = tid >> 4, pp = tid & 15;
    const bool full = (pp & 3) != 3;
    const unsigned xo = (unsigned)((pp >> 2) * 2048 + row * 64 + (pp & 3) * 16);
    unsigned xa[2][2];
#pragma unroll
    for (int k = 0; k < 2; ++k)
#pragma unroll
        for (int j = 0; j < 2; ++j) {
            const int Q = 28 * k + 7 * (pp >> 2) + 2 * (pp & 3) + j;
            xa[k][j] = XA_OFF + (unsigned)(((Q >> 3) * 2 + (row >> 4)) * 1024 + (((Q >> 1) & 3) * 16 + (row & 15)) * 16 + (Q & 1) * 8);
        }
    const int qt = w & 1, kq = w >> 1;
    const float scl = 0.18898223650461362f * LOG2E;
    u32x4 xv[4];
#pragma unroll
    for (int k = 0; k < 4; ++k) xv[k] = ld16(ws, xo, SB + L_XI + k * 8192u);

    for (int s = 0; s < NSEG; ++s) {
        TR(0);
        const unsigned xb = SB + L_XI + (unsigned)s * XI_SZ;
        for (unsigned spins = 0; !__all(ok4(xv[0]) & ok4(xv[1]) & ok4(xv[2]) & ok4(xv[3]));) {
            if (!retry(ws, xb + (unsigned)w * 256u, spins)) return;
#pragma unroll
            for (int k = 0; k < 4; ++k) xv[k] = ld16(ws, xo, xb + k * 8192u);
        }
        TR(1);
        {
            float f[4][8]; float sum = 0.f;
#pragma unroll
            for (int k = 0; k < 4; ++k) {
#pragma unroll
                for (int j = 0; j < 4; ++j) { f[k][2 * j] = bflo(xv[k][j]); f[k][2 * j + 1] = bfhi(xv[k][j]); }
#pragma unroll
                for (int j = 0; j < 8; ++j) sum += f[k][j];
            }
            const float mean = row16_sum(sum) * (1.0f / DM);
            float sq = 0.f, sq2 = 0.f;
#pragma unroll
            for (int k = 0; k < 4; ++k) {
#pragma unroll
                for (int j = 0; j < 4; ++j) { f[k][j] -= mean; sq += f[k][j] * f[k][j]; }
#pragma unroll
                for (int j = 4; j < 8; ++j) { f[k][j] -= mean; sq2 += f[k][j] * f[k][j]; }
            }
            const float rstd = 1.0f / sqrtf(row16_sum(sq + (full ? sq2 : 0.f)) * (1.0f / DM) + 1e-5f);
#pragma unroll
            for (int k = 0; k < 4; ++k) {
                const unsigned ko = (unsigned)(k >> 1) * 14336u;
                sts8(lds, xa[k & 1][0] + ko, (u32x2){pk2(f[k][0] * rstd, f[k][1] * rstd), pk2(f[k][2] * rstd, f[k][3] * rstd)});
                if (full) sts8(lds, xa[k & 1][1] + ko, (u32x2){pk2(f[k][4] * rstd, f[k][5] * rstd), pk2(f[k][6] * rstd, f[k][7] * rstd)});
            }
        }
        {
            const unsigned xn = s + 1 < NSEG ? xb + XI_SZ : xb;
#pragma unroll
            for (int k = 0; k < 4; ++k) xv[k] = ld16(ws, xo, xn + k * 8192u);
        }
        __syncthreads();
        TR(2);
        const int slot = s & 15;
        if (w < 4) {
            f32x4 acc[2];
            acc[0] = ZERO4; acc[1] = ZERO4;
#pragma unroll
            for (int kk = 0; kk < 14; ++kk)
#pragma unroll
                for (int mt = 0; mt < 2; ++mt)
                    acc[mt] = mfma(wr[kk], as8(lds16(lds, XA_OFF + (unsigned)((kk * 2 + mt) * 1024 + l * 16))), acc[mt]);
            const unsigned base = w < 2 ? Q_OFF : K_OFF + (unsigned)slot * 32u * RB;
#pragma unroll
            for (int mt = 0; mt < 2; ++mt) sts8(lds, base + (unsigned)((mt * 16 + r16) * RB + (w & 1) * 32 + g * 8), pk4(acc[mt]));
        } else {
            const unsigned mt = (unsigned)(w >> 1) & 1u;
            f32x4 acc = ZERO4;
#pragma unroll
            for (int kk = 0; kk < 14; ++kk)
                acc = mfma(as8(lds16(lds, XA_OFF + mt * 1024u + (unsigned)(kk * 2048 + l * 16))), wr[kk], acc);
            sts8(lds, V_OFF + (unsigned)(((w & 1) * 16 + r16) * VRB + slot * 64 + g * 16) + mt * 8u, pk4(acc));
        }
        __syncthreads();
        TR(3);
        {
            const bf16x8 qf = as8(lds16(lds, Q_OFF + (unsigned)((qt * 16 + r16) * RB + g * 16)));
            int qd = qt * 16 + r16 - 4 * g;
            asm volatile("" : "+v"(qd));
            const unsigned kl = K_OFF + (unsigned)(r16 * RB + g * 16), vl = V_OFF + (unsigned)(r16 * VRB + g * 16);
            f32x4 sc[2][2];
            float mx = -3.0e38f;
#pragma unroll
            for (int jj = 0; jj < 2; ++jj) {
                const int j = kq + 4 * jj;
                const unsigned sl = (unsigned)(s - j) & 15u;
#pragma unroll
                for (int kt = 0; kt < 2; ++kt) {
                    f32x4 c = mfma(as8(lds16(lds, kl + (sl * 32u + kt * 16u) * RB)), qf, ZERO4);
                    if (j > s) c = (f32x4){-3.0e38f, -3.0e38f, -3.0e38f, -3.0e38f}; else c *= scl;
                    sc[jj][kt] = c;
                }
            }
            if (kq == 0) {
                const unsigned sl = (unsigned)(s - 8) & 15u;
#pragma unroll
                for (int kt = 0; kt < 2; ++kt) {
                    f32x4 c = mfma(as8(lds16(lds, kl + (sl * 32u + kt * 16u) * RB)), qf, ZERO4);
#pragma unroll
                    for (int r = 0; r < 4; ++r) {
                        const float far_ = s >= 8 ? c[r] * scl : -3.0e38f;
                        sc[0][kt][r] = qd >= kt * 16 + r ? sc[0][kt][r] : far_;
                    }
                }
            }
#pragma unroll
            for (int jj = 0; jj < 2; ++jj)
#pragma unroll
                for (int kt = 0; kt < 2; ++kt)
                    mx = fmaxf(mx, fmaxf(fmaxf(sc[jj][kt][0], sc[jj][kt][1]), fmaxf(sc[jj][kt][2], sc[jj][kt][3])));
            mx = xg_max(mx);
            float sum = 0.f;
            f32x4 o[2];
            o[0] = ZERO4; o[1] = ZERO4;
#pragma unroll
            for (int jj = 0; jj < 2; ++jj) {
                const int j = kq + 4 * jj;
                const unsigned sl = (unsigned)(s - j) & 15u;
                f32x4 e[2];
#pragma unroll
                for (int kt = 0; kt < 2; ++kt)
#pragma unroll
                    for (int r = 0; r < 4; ++r) {
                        e[kt][r] = sc[jj][kt][r] > -1.0e38f ? __builtin_amdgcn_exp2f(sc[jj][kt][r] - mx) : 0.f;
                        sum += e[kt][r];
                    }
                if (jj == 0 && kq == 0) {
                    f32x4 e8[2];
#pragma unroll
                    for (int kt = 0; kt < 2; ++kt)
#pragma unroll
                        for (int r = 0; r < 4; ++r) {
                            const bool near_ = qd >= kt * 16 + r;
                            e8[kt][r] = near_ ? 0.f : e[kt][r]; e[kt][r] = near_ ? e[kt][r] : 0.f;
                        }
                    if (s >= 8) {
                        const unsigned s8 = (unsigned)(s - 8) & 15u;
                        const bf16x8 pf = cat8(pk4(e8[0]), pk4(e8[1]));
#pragma unroll
                        for (int dt = 0; dt < 2; ++dt) o[dt] = mfma(as8(lds16(lds, vl + dt * 16u * VRB + s8 * 64u)), pf, o[dt]);
                    }
                }
                if (j <= s) {
                    const bf16x8 pf = cat8(pk4(e[0]), pk4(e[1]));
#pragma unroll
                    for (int dt = 0; dt < 2; ++dt) o[dt] = mfma(as8(lds16(lds, vl + dt * 16u * VRB + sl * 64u)), pf, o[dt]);
                }
            }
            sum = xg_sum(sum);
            const unsigned mb = MG_OFF + (unsigned)w * MGW;
            stsf4(lds, mb + l * 16u, o[0]); stsf4(lds, mb + 1024u + l * 16u, o[1]);
            if (l < 16) sts8(lds, mb + 2048u + l * 8u, (u32x2){ubits(mx), ubits(sum)});
        }
        __syncthreads();
        TR(4);
        if (w < 4) {
            const int dt = w >> 1;
            float m4[4], l4[4]; float M = -3.0e38f;
#pragma unroll
            for (int k = 0; k < 4; ++k) {
                const u32x2 ml = lds8(lds, MG_OFF + (unsigned)(2 * k + qt) * MGW + 2048u + r16 * 8u);
                m4[k] = fbits(ml.x); l4[k] = fbits(ml.y); M = fmaxf(M, m4[k]);
            }
            float lsum = 0.f; f32x4 o = ZERO4;
#pragma unroll
            for (int k = 0; k < 4; ++k) {
                const float f = m4[k] > -1.0e38f ? __builtin_amdgcn_exp2f(m4[k] - M) : 0.f;
                lsum += l4[k] * f;
                o += ldsf4(lds, MG_OFF + (unsigned)(2 * k + qt) * MGW + (unsigned)(dt * 1024 + l * 16)) * f;
            }
            o *= __builtin_amdgcn_rcpf(lsum);
            if (dt == 1 && g == 3) o = ZERO4;
            st8(ws, (unsigned)((qt * 16 + r16) * 64 + dt * 32 + g * 8), SB + L_O + (unsigned)s * O_SZ + (unsigned)h * 2048u, pk4s(o));
            TR(6);
        }
    }
}

__global__ void __launch_bounds__(NT, 2) k_main(Params p) {
    extern __shared__ __attribute__((aligned(16))) unsigned char lds[];
    const rsrc_t ws = __builtin_amdgcn_make_buffer_rsrc(p.ws, 0, WS_NEED, 0x00020000);
    const int b = blockIdx.x, x = b & 7, y = b >> 3;
    if (x < 2) {
        if (y < 16) role_ms(p, ws, x, y, lds); else role_v(p, ws, x, y - 16, lds);
    } else {
        const int L = x >= 5 ? 1 : 0, z = x - 2 - 3 * L;
        if (z == 0) role_ts(p, ws, L, y, lds);
        else if (z == 1) { if (y < 28) role_vu(p, ws, L, y, lds); else role_wo(p, ws, L, y - 28, lds); }
        else { if (y < 16) role_at(p, ws, L, y, lds); else role_vo(p, ws, L, y - 16, lds); }
    }
}

__global__ void __launch_bounds__(NT, 2) k_pro(Params p) {
    extern __shared__ __attribute__((aligned(16))) unsigned char lds[];
    const int b = blockIdx.x, tid = threadIdx.x, w = __builtin_amdgcn_readfirstlane(tid >> 6), l = tid & 63, g = l >> 4, r16 = l & 15;
    if (b < 224) {
        const bool isB = b >= 112;
        const int bb = isB ? b - 112 : b, h = bb / 7, nb = bb % 7;
        constexpr unsigned RS = 528;
        if (isB) {
#pragma unroll
            for (int k = 0; k < 4; ++k) {
                const int u = tid + 512 * k, e = u >> 3, c8 = u & 7;
                const u32x4 v = *(const u32x4*)(p.Wo + ((size_t)h * 256 + e) * DM + nb * 64 + c8 * 8);
#pragma unroll
                for (int j = 0; j < 4; ++j) {
                    *(uint16_t*)(lds + (c8 * 8 + 2 * j) * RS + e * 2) = (uint16_t)(v[j] & 0xFFFFu);
                    *(uint16_t*)(lds + (c8 * 8 + 2 * j + 1) * RS + e * 2) = (uint16_t)(v[j] >> 16);
                }
            }
        } else {
#pragma unroll
            for (int k = 0; k < 4; ++k) {
                const int u = tid + 512 * k, n = u >> 5, e8 = u & 31;
                sts16(lds, n * RS + e8 * 16, *(const u32x4*)(p.Wk + ((size_t)(nb * 64 + n) * 16 + h) * 256 + e8 * 8));
            }
        }
        __syncthreads();
        if (w == 7) {
            const u32x4 sent = {SENT, SENT, SENT, SENT};
            constexpr unsigned KB0 = L_O / 1024, KBM = L_MEM / 1024, KBL = L_ARMED / 1024;
            for (unsigned k = KB0 + (unsigned)b; k < 2 * KBL; k += 224u) {
                const unsigned L = k >= KBL ? 1u : 0u, kb = k - L * KBL;
                if (kb >= KBM && kb < KBM + MEM_SZ / 1024) continue;
                *(u32x4*)(p.ws + OFF_ST + (size_t)L * L_TOTAL + (size_t)kb * 1024 + l * 16) = sent;
            }
        }
        if (w < 7) {
            const uint16_t* Wd = isB ? p.Wv : p.Wq;
            f32x4 acc[4][4];
#pragma unroll
            for (int i = 0; i < 4; ++i)
#pragma unroll
                for (int j = 0; j < 4; ++j) acc[i][j] = (f32x4){0.f, 0.f, 0.f, 0.f};
#pragma unroll
            for (int half = 0; half < 2; ++half) {
                u32x4 af[4][4];
#pragma unroll
                for (int ke = 0; ke < 4; ++ke)
#pragma unroll
                    for (int i = 0; i < 4; ++i)
                        af[ke][i] = *(const u32x4*)(Wd + ((size_t)(w * 64 + i * 16 + r16) * 16 + h) * 256 + (half * 4 + ke) * 32 + g * 8);
#pragma unroll
                for (int ke = 0; ke < 4; ++ke)
#pragma unroll
                    for (int j = 0; j < 4; ++j) {
                        const bf16x8 bfr_ = as8(lds16(lds, (unsigned)((j * 16 + r16) * RS + (half * 4 + ke) * 64 + g * 16)));
#pragma unroll
                        for (int i = 0; i < 4; ++i) acc[i][j] = mfma(as8(af[ke][i]), bfr_, acc[i][j]);
                    }
            }
            const float sc = isB ? 1.0f : 0.0625f;
#pragma unroll
            for (int i = 0; i < 4; ++i)
#pragma unroll
                for (int j = 0; j < 4; ++j) {
                    const int nt = nb * 4 + j, d0 = w * 64 + i * 16 + 4 * g, kk = d0 >> 5;
                    const size_t tile = isB ? (size_t)(nt * 16 + h) : (size_t)(h * 28 + nt);
                    unsigned char* dst = p.ws + (isB ? OFF_FB : OFF_FA) + (tile * 14 + kk) * 1024 + (((d0 >> 3) & 3) * 16 + r16) * 16 + (d0 & 7) * 2;
                    *(u32x2*)dst = pk4(acc[i][j] * sc);
                }
        }
        return;
    }
    const int gt = (b - 224) * NT + tid;
    for (int u = gt; u < 16 * 6 * 14 * 64; u += 32 * NT) {
        const int ln = u & 63, kk = (u >> 6) % 14, t = (u / 896) % 6, h = u / 5376;
        const int cl = (t & 1) * 16 + (ln & 15), d0 = kk * 32 + (ln >> 4) * 8;
        u32x4 o = {0u, 0u, 0u, 0u};
        if (cl < 28) {
            const uint16_t* src = p.wqkv + (size_t)d0 * 1344 + (t >> 1) * 448 + h * 28 + cl;
#pragma unroll
            for (int j = 0; j < 4; ++j) o[j] = (unsigned)src[(2 * j) * 1344] | ((unsigned)src[(2 * j + 1) * 1344] << 16);
        }
        *(u32x4*)(p.ws + OFF_FQ + (size_t)u * 16) = o;
    }
    for (int u = gt; u < 28 * 14 * 64; u += 32 * NT) {
        const int ln = u & 63, kk = (u >> 6) % 14, t = u / 896;
        const uint16_t* src = p.wo + (size_t)(kk * 32 + (ln >> 4) * 8) * DM + t * 16 + (ln & 15);
        u32x4 o;
#pragma unroll
        for (int j = 0; j < 4; ++j) o[j] = (unsigned)src[(2 * j) * DM] | ((unsigned)src[(2 * j + 1) * DM] << 16);
        *(u32x4*)(p.ws + OFF_FW + (size_t)u * 16) = o;
    }
    for (int u = gt; u < NSEG * 2048; u += 32 * NT) {
        const int u4 = u & 3, row = (u >> 2) & 31, v = (u >> 7) & 15, s = u >> 11;
        const uint16_t* src = p.x + (size_t)(s * 32 + row) * DM + v * 28 + u4 * 8;
        const u32x2 a = *(const u32x2*)src;
        u32x2 c = {0u, 0u};
        if (u4 != 3) c = *(const u32x2*)(src + 4);
        *(u32x4*)(p.ws + OFF_ST + L_XI + (size_t)u * 16) = (u32x4){nosent(a.x), nosent(a.y), nosent(c.x), nosent(c.y)};
    }
    if (gt < 2 * 16 * 7 * 8) {
        const int L = gt >= 896 ? 1 : 0, e = gt - L * 896, i = e & 7, Q = e >> 3, v = Q / 7, q = Q % 7;
        u32x2 m = *(const u32x2*)(p.mem0 + i * DM + Q * 4);
        m.x = nosent(m.x); m.y = nosent(m.y);
        *(u32x2*)(p.ws + OFF_ST + (size_t)L * L_TOTAL + L_MEM + v * 512 + q * 64 + i * 8) = m;
    }
    for (unsigned u = gt; u < (OFF_FLG + SZ_FLG) / 16; u += 32 * NT) *(u32x4*)(p.ws + (size_t)u * 16) = (u32x4){0u, 0u, 0u, 0u};
}

}

extern "C" void kernel_launch(void* const* d_in, const int* in_sizes, int n_in, void* d_out, int out_size, void* d_ws, size_t ws_size,
                              hipStream_t stream) {
    Params p;
    p.x = (const uint16_t*)d_in[0]; p.wqkv = (const uint16_t*)d_in[1]; p.wo = (const uint16_t*)d_in[2];
    p.mem0 = (const uint16_t*)d_in[4]; p.Wq = (const uint16_t*)d_in[5]; p.Wk = (const uint16_t*)d_in[6];
    p.Wv = (const uint16_t*)d_in[7]; p.Wo = (const uint16_t*)d_in[8];
    p.out = (uint16_t*)d_out; p.ws = (unsigned char*)d_ws;
    constexpr int LDS_PRO = 64 * 528;
    (void)hipFuncSetAttribute((const void*)k_main, hipFuncAttributeMaxDynamicSharedMemorySize, LDS_MAIN);
    hipLaunchKernelGGL(k_pro, dim3(256), dim3(NT), LDS_PRO, stream, p);
    hipLaunchKernelGGL(k_main, dim3(256), dim3(NT), LDS_MAIN, stream, p);
}
```

```cpp
#include <hip/hip_runtime.h>
#include <hip/hip_bf16.h>
#include <stdint.h>

namespace {

typedef __bf16 bf16x2 __attribute__((ext_vector_type(2)));
typedef __bf16 bf16x8 __attribute__((ext_vector_type(8)));
typedef __bf16 bf16x16 __attribute__((ext_vector_type(16)));
typedef short s16x4 __attribute__((ext_vector_type(4)));
typedef float f32x4 __attribute__((ext_vector_type(4)));
typedef unsigned u32x2 __attribute__((ext_vector_type(2)));
typedef unsigned u32x4 __attribute__((ext_vector_type(4)));
typedef unsigned u32x8 __attribute__((ext_vector_type(8)));
#define LAS __attribute__((address_space(3)))
#define DI __device__ __forceinline__

constexpr int DM = 448, NSEG = 64, NT = 512;
constexpr unsigned SENT = 0xFFFFFFFFu;
constexpr unsigned SPIN_CAP = 1u << 17;

constexpr unsigned OFF_CTRL = 0;
constexpr unsigned OFF_FLG = 4096, FLG_U = 0, FLG_Y = 28u * 8 * 128, FLG_M = FLG_Y + 16u * 8 * 128, FLG_L = 49152, SZ_FLG = 2 * FLG_L;
constexpr unsigned OFF_FA = OFF_FLG + SZ_FLG, SZ_FA = 16u * 28 * 14 * 1024;
constexpr unsigned OFF_FB = OFF_FA + SZ_FA;
constexpr unsigned OFF_FQ = OFF_FB + SZ_FA, SZ_FQ = 16u * 6 * 16 * 1024;
constexpr unsigned OFF_FW = OFF_FQ + SZ_FQ, SZ_FW = 28u * 14 * 1024;
constexpr unsigned OFF_ST = OFF_FW + SZ_FW;
constexpr unsigned XI_SZ = 32768, XN_SZ = 28672, O_SZ = 32768, MEM_SZ = 8192, PM_SZ = 12288, PT_SZ = 40960, U_SZ = 458752, Y_SZ = 131072;
constexpr unsigned L_XI = 0, L_O = L_XI + 64u * XI_SZ, L_XN = L_O + 64u * O_SZ, L_XMID = L_XN + 64u * XN_SZ,
                   L_MEM = L_XMID + 64u * XN_SZ, L_PM = L_MEM + 64u * MEM_SZ, L_PT = L_PM + 64u * PM_SZ, L_ARMED = L_PT + 64u * PT_SZ,
                   L_U = L_ARMED, L_Y = L_U + 64u * U_SZ, L_TOTAL = L_Y + 64u * Y_SZ;
constexpr unsigned WS_NEED = OFF_ST + 2u * L_TOTAL;
constexpr int LDS_MAIN = 160 * 1024;

struct Params {
    const uint16_t* x;
    const uint16_t* wqkv;
    const uint16_t* wo;
    const uint16_t* mem0;
    const uint16_t* Wq;
    const uint16_t* Wk;
    const uint16_t* Wv;
    const uint16_t* Wo;
    uint16_t* out;
    unsigned char* ws;
};

typedef __amdgpu_buffer_rsrc_t rsrc_t;
DI u32x4 ld16(rsrc_t r, unsigned vo, unsigned so) { return __builtin_amdgcn_raw_buffer_load_b128(r, vo, so, 16); }
DI u32x2 ld8(rsrc_t r, unsigned vo, unsigned so) { return __builtin_amdgcn_raw_buffer_load_b64(r, vo, so, 16); }
DI unsigned ld4(rsrc_t r, unsigned vo, unsigned so) { return __builtin_amdgcn_raw_buffer_load_b32(r, vo, so, 16); }
DI void st16(rsrc_t r, unsigned vo, unsigned so, u32x4 v) { __builtin_amdgcn_raw_buffer_store_b128(v, r, vo, so, 16); }
DI void st8(rsrc_t r, unsigned vo, unsigned so, u32x2 v) { __builtin_amdgcn_raw_buffer_store_b64(v, r, vo, so, 16); }
DI void st4(rsrc_t r, unsigned vo, unsigned so, unsigned v) { __builtin_amdgcn_raw_buffer_store_b32(v, r, vo, so, 16); }
DI bool ok4(u32x4 v) { return (v.x != SENT) & (v.y != SENT) & (v.z != SENT) & (v.w != SENT); }
DI bool ok2(u32x2 v) { return (v.x != SENT) & (v.y != SENT); }
DI void drain() { asm volatile("s_waitcnt vmcnt(0)" ::: "memory"); }
DI unsigned uni(unsigned v) { return (unsigned)__builtin_amdgcn_readfirstlane((int)v); }

DI bool spin_ok(unsigned& spins, rsrc_t ws) {
    asm volatile("" ::: "memory");
    ++spins;
    if ((spins & 63u) == 0u) {
        if (spins > SPIN_CAP) { st4(ws, 0, OFF_CTRL, 1u); return false; }
        if (uni(ld4(ws, 0, OFF_CTRL)) != 0u) return false;
    }
    return true;
}
DI bool probe(rsrc_t ws, unsigned so, unsigned& spins) {
    for (;;) {
        __builtin_amdgcn_s_sleep(4);
        if (uni(ld4(ws, 0, so)) != SENT) return true;
        if (!spin_ok(spins, ws)) return false;
    }
}
DI bool retry(rsrc_t ws, unsigned so, unsigned& spins) {
    if (spins == 0u) { spins = 1u; return probe(ws, so, spins); }
    return spin_ok(spins, ws);
}
DI bool wait_flag(rsrc_t ws, unsigned vo, unsigned need, unsigned& have) {
    for (unsigned spins = 0; have < need;) {
        const unsigned v = ld4(ws, vo, OFF_FLG);
        const unsigned a = __builtin_amdgcn_readlane(v, 0), b = __builtin_amdgcn_readlane(v, 1), c = __builtin_amdgcn_readlane(v, 2);
        have = min(a, min(b, c));
        if (have >= need) break;
        __builtin_amdgcn_s_sleep(2);
        if (!spin_ok(spins, ws)) return false;
    }
    asm volatile("" ::: "memory");
    return true;
}
DI void post_flag(rsrc_t ws, unsigned so, unsigned v, int l) { if (l == 0) st4(ws, 0, OFF_FLG + so, v); }

DI unsigned pk2(float a, float b) { bf16x2 v = {(__bf16)a, (__bf16)b}; return __builtin_bit_cast(unsigned, v); }
DI unsigned nosent(unsigned x) { return x < 0xFFFFFFFEu ? x : 0xFFFFFFFEu; }
DI u32x2 pk4(f32x4 v) { return (u32x2){pk2(v[0], v[1]), pk2(v[2], v[3])}; }
DI u32x2 pk4s(f32x4 v) { return (u32x2){nosent(pk2(v[0], v[1])), nosent(pk2(v[2], v[3]))}; }
DI float bflo(unsigned x) { return __builtin_bit_cast(float, x << 16); }
DI float bfhi(unsigned x) { return __builtin_bit_cast(float, x & 0xFFFF0000u); }
DI float bfr(float x) { return (float)(__bf16)x; }
DI bf16x8 as8(u32x4 v) { return __builtin_bit_cast(bf16x8, v); }
DI bf16x8 cat8(u32x2 a, u32x2 b) { return as8((u32x4){a.x, a.y, b.x, b.y}); }
DI bf16x16 cat16(u32x4 a, u32x4 b) { return __builtin_bit_cast(bf16x16, (u32x8){a.x, a.y, a.z, a.w, b.x, b.y, b.z, b.w}); }
DI f32x4 mfma(bf16x8 a, bf16x8 b, f32x4 c) { return __builtin_amdgcn_mfma_f32_16x16x32_bf16(a, b, c, 0, 0, 0); }
DI f32x4 mfma16(u32x2 a, u32x2 b, f32x4 c) {
    return mfma(as8((u32x4){a.x, a.y, 0u, 0u}), as8((u32x4){b.x, b.y, 0u, 0u}), c);
}
DI f32x4 smfma(bf16x8 a, bf16x16 b, f32x4 c, int idx) { return __builtin_amdgcn_smfmac_f32_16x16x64_bf16(a, b, c, idx, 0, 0); }
template <int CTRL> DI float dpp(float v) {
    return __builtin_bit_cast(float, __builtin_amdgcn_update_dpp(0, __builtin_bit_cast(int, v), CTRL, 0xf, 0xf, true));
}
DI float row16_max(float v) {
    v = fmaxf(v, dpp<0x128>(v)); v = fmaxf(v, dpp<0x124>(v)); v = fmaxf(v, dpp<0x122>(v)); v = fmaxf(v, dpp<0x121>(v)); return v;
}
DI float row16_sum(float v) {
    v += dpp<0x128>(v); v += dpp<0x124>(v); v += dpp<0x122>(v); v += dpp<0x121>(v); return v;
}
DI float fbits(unsigned v) { return __builtin_bit_cast(float, v); }
DI unsigned ubits(float v) { return __builtin_bit_cast(unsigned, v); }
DI float half_add(float a, float b) {
    const auto q = __builtin_amdgcn_permlane32_swap(ubits(a), ubits(b), false, false);
    return fbits(q[0]) + fbits(q[1]);
}
DI float xg_max(float x) {
    auto q = __builtin_amdgcn_permlane32_swap(ubits(x), ubits(x), false, false);
    x = fmaxf(fbits(q[0]), fbits(q[1]));
    q = __builtin_amdgcn_permlane16_swap(ubits(x), ubits(x), false, false);
    return fmaxf(fbits(q[0]), fbits(q[1]));
}
DI float xg_sum(float x) {
    auto q = __builtin_amdgcn_permlane32_swap(ubits(x), ubits(x), false, false);
    x = fbits(q[0]) + fbits(q[1]);
    q = __builtin_amdgcn_permlane16_swap(ubits(x), ubits(x), false, false);
    return fbits(q[0]) + fbits(q[1]);
}
DI u32x4 lds16(const unsigned char* lds, unsigned off) { return *(const u32x4*)(lds + off); }
DI u32x2 lds8(const unsigned char* lds, unsigned off) { return *(const u32x2*)(lds + off); }
DI f32x4 ldsf4(const unsigned char* lds, unsigned off) { return *(const f32x4*)(lds + off); }
DI void sts16(unsigned char* lds, unsigned off, u32x4 v) { *(u32x4*)(lds + off) = v; }
DI void sts8(unsigned char* lds, unsigned off, u32x2 v) { *(u32x2*)(lds + off) = v; }
DI void sts4(unsigned char* lds, unsigned off, unsigned v) { *(unsigned*)(lds + off) = v; }
DI void stsf4(unsigned char* lds, unsigned off, f32x4 v) { *(f32x4*)(lds + off) = v; }
DI u32x2 lds_tr(unsigned char* lds, unsigned off) {
    return __builtin_bit_cast(u32x2, __builtin_amdgcn_ds_read_tr16_b64_v4i16((LAS s16x4*)(lds + off)));
}
constexpr float LOG2E = 1.4426950408889634f;
#define ZERO4 ((f32x4){0.f, 0.f, 0.f, 0.f})

#define TR(ev) do {} while (0)
#define ISO(s) (s)
#define STAT(i, val) do {} while (0)
#define TW(ev) do {} while (0)
struct XnRegs { u32x4 v[4]; };
DI void xn_issue(rsrc_t ws, unsigned src, int tid, XnRegs& x) {
    x.v[0] = ld16(ws, tid * 16u, src); x.v[1] = ld16(ws, tid * 16u, src + 8192u); x.v[2] = ld16(ws, tid * 16u, src + 16384u);
    x.v[3] = ld16(ws, (tid & 255) * 16u, src + 24576u);
}
DI bool xn_ok(const XnRegs& x) { return __all(ok4(x.v[0]) & ok4(x.v[1]) & ok4(x.v[2]) & ok4(x.v[3])); }
DI void xn_stage(const XnRegs& x, unsigned char* lds, unsigned xn_off, int tid) {
    sts16(lds, xn_off + tid * 16u, x.v[0]); sts16(lds, xn_off + tid * 16u + 8192u, x.v[1]); sts16(lds, xn_off + tid * 16u + 16384u, x.v[2]);
    if (tid < 256) sts16(lds, xn_off + (1536u + tid) * 16u, x.v[3]);
}
DI bool fetch_xn(rsrc_t ws, unsigned src, unsigned char* lds, unsigned xn_off, int tid, int w, int slot) {
    XnRegs x;
    for (unsigned spins = 0;;) {
        xn_issue(ws, src, tid, x);
        if (xn_ok(x)) break;
        if (!retry(ws, src + (unsigned)w * 1024u, spins)) return false;
    }
    xn_stage(x, lds, xn_off, slot);
    return true;
}
template <bool WS_, bool WK_, bool HOT>
DI bool fetch_mem(rsrc_t ws, unsigned src, unsigned char* lds, unsigned ms_off, unsigned mk_off, int tid, int w) {
    const int v = tid >> 5, q0 = (tid >> 2) & 7, rp = tid & 3;
    const int q = q0 == 7 ? 6 : q0;
    const unsigned off = (unsigned)(v * 512 + q * 64 + rp * 16);
    u32x4 m;
    for (unsigned spins = 0;;) {
        m = ld16(ws, off, src);
        if (__all(ok4(m))) break;
        if (HOT) { if (!spin_ok(spins, ws)) return false; }
        else if (!retry(ws, src + (unsigned)w * 1024u, spins)) return false;
    }
    if (q0 != 7) {
        const int Q = 7 * v + q;
        if (WK_) sts16(lds, mk_off + (unsigned)(Q * 64 + rp * 16), m);
        if (WS_) {
            const unsigned base = ms_off + (unsigned)((Q >> 4) * 1024 + ((Q >> 2) & 3) * 256 + (Q & 3) * 4 + rp * 32);
            sts4(lds, base, m.x); sts4(lds, base + 128, m.y); sts4(lds, base + 16, m.z); sts4(lds, base + 144, m.w);
        }
    }
    return true;
}
#define PRE_MS_MEM 2800
#define PRE_V_PM 4000
#define PRE_V_MEM 900
#define GAP 5
#define XN_DELAY 9
DI unsigned now() { return (unsigned)__builtin_amdgcn_s_memtime(); }
DI void sleep_until(unsigned t0, unsigned pre) { while (now() - t0 < pre) __builtin_amdgcn_s_sleep(1); }
#define TGT_MEM 800
#define TGT_PM 1000
#define ST_SHIFT 2
DI unsigned stamp() { return ((unsigned)__builtin_amdgcn_s_memrealtime() & 0x3FFFFFFFu) | 0x40000000u; }
DI unsigned later(unsigned a, unsigned b) { return (int)((a - b) << 2) > 0 ? a : b; }
DI bool pace(unsigned t0, unsigned pre) {
    if (now() - t0 >= pre) return false;
    while (now() - t0 < pre) __builtin_amdgcn_s_sleep(1);
    return true;
}
DI unsigned steer(unsigned pre, bool waited, unsigned r0, unsigned ta, unsigned tb, int target) {
    if (ta == 0u || tb == 0u) return pre;
    const int sa = (int)((r0 - ta) << 2) >> 2, sb = (int)((r0 - tb) << 2) >> 2;
    const int err = target - max(min(min(sa, sb), 1000), -1000) * 23;
    if (err < 0 && !waited) return pre;
    const int np = (int)pre + max(min(err >> ST_SHIFT, 256), -256);
    return (unsigned)max(np, 0);
}
#define HOT_GAP 0
#define LAZY_GAP 4
DI unsigned mx4(u32x4 v) { return max(max(v.x, v.y), max(v.z, v.w)); }
DI bool none_sent(unsigned t) { return __builtin_amdgcn_ballot_w64(t == SENT) == 0ull; }
template <bool WS_, bool WK_>
DI void mem_stage(u32x4 m, unsigned char* lds, unsigned ms_off, unsigned mk_off, int tid) {
    const int v = tid >> 5, q = (tid >> 2) & 7, rp = tid & 3;
    if (q != 7) {
        const int Q = 7 * v + q;
        if (WK_) sts16(lds, mk_off + (unsigned)(Q * 64 + rp * 16), m);
        if (WS_) {
            const unsigned base = ms_off + (unsigned)((Q >> 4) * 1024 + ((Q >> 2) & 3) * 256 + (Q & 3) * 4 + rp * 32);
            sts4(lds, base, m.x); sts4(lds, base + 128, m.y); sts4(lds, base + 16, m.z); sts4(lds, base + 144, m.w);
        }
    }
}
DI unsigned mem_unit(int tid) { const int q0 = (tid >> 2) & 7; return (unsigned)((tid >> 5) * 512 + (q0 == 7 ? 6 : q0) * 64 + (tid & 3) * 16); }
DI u32x2 xn_key(const unsigned char* lds, unsigned xn_off, int nt, int mt, int g, int r16) {
    return lds8(lds, xn_off + (unsigned)(((nt >> 1) * 2 + mt) * 1024 + ((2 * (nt & 1) + (g >> 1)) * 16 + r16) * 16 + (g & 1) * 8));
}
DI u32x2 mem_key(const unsigned char* lds, unsigned mk_off, int nt, int g, int r16) {
    return lds8(lds, mk_off + (unsigned)(((4 * nt + g) * 8 + (r16 & 7)) * 8));
}

#define SCHED() __builtin_amdgcn_sched_barrier(0)
template <int HI>
DI void ms_scores(unsigned char* lds, const bf16x16 (&wr)[3][7], const u32x4 (&wsh)[4], const u32x4 (&kt0)[2], unsigned xw, int sidx,
                  unsigned mab, unsigned ttw, unsigned ttr, unsigned kx1, unsigned km0, unsigned km1, f32x4 (&sa)[3]) {
    constexpr int NS = HI ? 3 : 4, NB = 7 - NS, K0 = HI ? 4 : 0, K1 = HI ? 0 : 4;
    constexpr int ORD[7] = {K0, K0 + 1, K0 + 2, HI ? 0 : 3, HI ? 1 : 4, HI ? 2 : 5, HI ? 3 : 6};
    bf16x8 ma[7];
#define MA(i) ma[ORD[i]] = as8(lds16(lds, mab + ORD[i] * 1024u))
#define T3(k) do { acc[0] = smfma(ma[k], wr[0][k], acc[0], sidx); acc[1] = smfma(ma[k], wr[1][k], acc[1], sidx); \
                   acc[2] = smfma(ma[k], wr[2][k], acc[2], sidx); } while (0)
    MA(0); MA(1); MA(2);
    f32x4 acc[4];
#pragma unroll
    for (int t = 0; t < 4; ++t) acc[t] = ZERO4;
    u32x4 w2[2], w3[2], kt1[2], kq0, kq1;
    SCHED();
    acc[3] = smfma(ma[K0], cat16(wsh[0], wsh[1]), acc[3], sidx);
    SCHED();
    w2[0] = lds16(lds, xw + 4096u); w2[1] = lds16(lds, xw + 5120u);
    SCHED();
    T3(K0);
    acc[3] = smfma(ma[K0 + 1], cat16(wsh[2], wsh[3]), acc[3], sidx);
    SCHED();
    MA(3);
    if (!HI) { w3[0] = lds16(lds, xw + 6144u); w3[1] = lds16(lds, xw + 7168u); } else { MA(4); MA(5); }
    SCHED();
    T3(K0 + 1);
    acc[3] = smfma(ma[K0 + 2], cat16(w2[0], w2[1]), acc[3], sidx);
    SCHED();
    if (!HI) { MA(4); MA(5); MA(6); } else { MA(6); kq0 = lds16(lds, km0); kq1 = lds16(lds, km1); }
    SCHED();
    T3(K0 + 2);
    if (!HI) {
        acc[3] = smfma(ma[3], cat16(w3[0], w3[1]), acc[3], sidx);
        SCHED();
        kq0 = lds16(lds, km0); kq1 = lds16(lds, km1);
        SCHED();
        T3(3);
    }
    SCHED();
    kt1[0] = lds16(lds, kx1); kt1[1] = lds16(lds, kx1 + 1024u);
    SCHED();
#pragma unroll
    for (int i = 0; i < NB; ++i) acc[0] = smfma(ma[K1 + i], wr[0][K1 + i], acc[0], sidx);
    acc[1] = smfma(ma[K1], wr[1][K1], acc[1], sidx);
    acc[1] = smfma(ma[K1 + 1], wr[1][K1 + 1], acc[1], sidx);
    SCHED();
    {
        f32x4 y;
#pragma unroll
        for (int r = 0; r < 4; ++r) y[r] = half_add(acc[3][r], acc[0][r]);
        sts8(lds, ttw, pk4(y));
    }
    const bf16x8 tb0 = cat8(lds_tr(lds, ttr), lds_tr(lds, ttr + 128u));
    SCHED();
#pragma unroll
    for (int i = 2; i < NB; ++i) acc[1] = smfma(ma[K1 + i], wr[1][K1 + i], acc[1], sidx);
#pragma unroll
    for (int i = 0; i < NB; ++i) acc[2] = smfma(ma[K1 + i], wr[2][K1 + i], acc[2], sidx);
    SCHED();
    sa[0] = mfma(as8(kq0), tb0, ZERO4); sa[1] = mfma(as8(kt0[0]), tb0, ZERO4); sa[2] = mfma(as8(kt0[1]), tb0, ZERO4);
    SCHED();
    {
        f32x4 y;
#pragma unroll
        for (int r = 0; r < 4; ++r) y[r] = half_add(acc[1][r], acc[2][r]);
        sts8(lds, ttw + 512u, pk4(y));
    }
    const bf16x8 tb1 = cat8(lds_tr(lds, ttr + 512u), lds_tr(lds, ttr + 640u));
    sa[1] = mfma(as8(kt1[0]), tb1, sa[1]); sa[2] = mfma(as8(kt1[1]), tb1, sa[2]); sa[0] = mfma(as8(kq1), tb1, sa[0]);
#undef MA
#undef T3
}
template <int HI>
DI void role_ms_t(const Params& p, rsrc_t ws, int L, int h, unsigned char* lds) {
    constexpr unsigned MS_OFF = 0, MK_OFF = 7168, SP_OFF = 14336, SPW = 1408, TT_OFF = 26624, XN_OFF = 34816, W_OFF = 63488;
    const int tid = threadIdx.x, w = __builtin_amdgcn_readfirstlane(tid >> 6), l = tid & 63, g = l >> 4, r16 = l & 15;
    const unsigned SB = OFF_ST + (unsigned)L * L_TOTAL;
    const u32x4* fa = (const u32x4*)(p.ws + OFF_FA + (size_t)h * 28 * 14 * 1024);
    bf16x16 wr[3][7];
#pragma unroll
    for (int t = 0; t < 3; ++t)
#pragma unroll
        for (int ks = 0; ks < 7; ++ks)
            wr[t][ks] = cat16(fa[((3 * w + t) * 14 + 2 * ks) * 64 + l], fa[((3 * w + t) * 14 + 2 * ks + 1) * 64 + l]);
#pragma unroll
    for (int k = 0; k < 7; ++k) sts16(lds, W_OFF + (tid + 512 * k) * 16u, fa[24 * 14 * 64 + tid + 512 * k]);
    __syncthreads();
    const int sidx = (r16 & 8) ? 0xEEEE : 0x4444;
    const unsigned ttw = TT_OFF + (unsigned)(w * 1024 + r16 * 32 + g * 8);
    const unsigned ttr = TT_OFF + (unsigned)(w * 1024 + (8 * (g & 1) + (r16 >> 2)) * 32 + ((r16 & 1) + 2 * (g >> 1)) * 8);
    const unsigned xw = W_OFF + ((unsigned)(w & 3) * 14u + (HI ? 8u : 0u)) * 1024u + l * 16u;
    const int n0 = (g >> 1) ? 3 * w : 24 + (w & 3), n1 = 3 * w + 1 + (g >> 1);
    const unsigned kx0 = XN_OFF + (unsigned)((n0 >> 1) * 2048 + ((n0 & 1) * 2 + (g & 1)) * 256 + r16 * 16);
    const unsigned kx1 = XN_OFF + (unsigned)((n1 >> 1) * 2048 + ((n1 & 1) * 2 + (g & 1)) * 256 + r16 * 16);
    const unsigned km0 = MK_OFF + (unsigned)((n0 * 2 + (g & 1)) * 128 + (r16 & 7) * 16);
    const unsigned km1 = MK_OFF + (unsigned)((n1 * 2 + (g & 1)) * 128 + (r16 & 7) * 16);
    const int mq = (tid >> 2) & 7, mQ = 7 * (tid >> 5) + mq;
    const unsigned mkw = MK_OFF + (unsigned)((mQ >> 1) * 128 + (tid & 3) * 32 + (mQ & 1) * 8);
    const unsigned spw = SP_OFF + (unsigned)w * SPW + (unsigned)(((r16 >> 2) & 1) * 704 + (r16 & 3) * 16);
    const unsigned spr = SP_OFF + (unsigned)((w & 1) * 704 + (l < 40 ? l : 32 + (l & 7)) * 16);
    const unsigned mu = (unsigned)tid * 16u;
    unsigned pre = PRE_MS_MEM;
    unsigned t0 = now() - PRE_MS_MEM;
    for (int s = 0; s < NSEG - 1; ++s) {
        TR(0);
        if (w >= 2) __builtin_amdgcn_s_sleep(XN_DELAY);
        STAT(0, st_a); STAT(1, st_b); STAT(2, st_c); STAT(3, st_d);
        if (!fetch_xn(ws, SB + L_XN + (unsigned)(ISO(s)) * XN_SZ, lds, XN_OFF, tid, w, tid)) return;
        __syncthreads();
        TR(1);
        u32x4 wsh[4], kt0[2];
#pragma unroll
        for (int k = 0; k < 4; ++k) wsh[k] = lds16(lds, xw + (unsigned)k * 1024u);
        kt0[0] = lds16(lds, kx0); kt0[1] = lds16(lds, kx0 + 1024u);
        {
            const unsigned src = SB + L_MEM + (unsigned)s * MEM_SZ;
            const bool waited = pace(t0, pre);
            const unsigned r0 = stamp();
            u32x4 m;
            unsigned spins = 0;
            for (;;) {
                m = ld16(ws, mu, src);
                mem_stage<true, false>(m, lds, MS_OFF, 0, tid);
                if (mq != 7) { sts8(lds, mkw, (u32x2){m.x, m.y}); sts8(lds, mkw + 16u, (u32x2){m.z, m.w}); }
                if (none_sent(mx4(m))) break;
                if (!spin_ok(spins, ws)) return;
            }
            const unsigned ta = __builtin_amdgcn_readlane(m.x, 28), tb = __builtin_amdgcn_readlane(m.x, 60);
            TR(2);
            __syncthreads();
            pre = steer(pre, waited, r0, ta, tb, TGT_MEM);
        }
        TR(3);
        f32x4 sa[3];
        ms_scores<HI>(lds, wr, wsh, kt0, xw, sidx, MS_OFF + l * 16u, ttw, ttr, kx1, km0, km1, sa);
        if (r16 < 8) {
            stsf4(lds, spw + (unsigned)g * 128u, sa[1]);
            stsf4(lds, spw + (unsigned)g * 128u + 64u, sa[2]);
            if (g < 2) stsf4(lds, spw + 512u + (unsigned)g * 64u, sa[0]);
        }
        TR(4);
        __syncthreads();
        TR(5);
        t0 = now();
        if (!HI && w < 2) {
            const unsigned ts = stamp();
            f32x4 q[8];
#pragma unroll
            for (int ww = 0; ww < 8; ++ww) q[ww] = ldsf4(lds, spr + (unsigned)ww * SPW);
            const f32x4 a = ((q[0] + q[1]) + (q[2] + q[3])) + ((q[4] + q[5]) + (q[6] + q[7]));
            float mx = fmaxf(fmaxf(a[0], a[1]), fmaxf(a[2], a[3]));
            mx = fmaxf(mx, dpp<0x124>(mx)); mx = fmaxf(mx, dpp<0x128>(mx));
            mx = xg_max(mx) * LOG2E;
            f32x4 e;
#pragma unroll
            for (int r = 0; r < 4; ++r) e[r] = __builtin_amdgcn_exp2f(__builtin_fmaf(a[r], LOG2E, -mx));
            float part = l < 40 ? (e[0] + e[1]) + (e[2] + e[3]) : 0.f;
            part += dpp<0x124>(part); part += dpp<0x128>(part);
            const float inv = __builtin_amdgcn_rcpf(xg_sum(part));
            u32x2 pa = pk4s(e * inv);
            const unsigned nx = (unsigned)__builtin_amdgcn_update_dpp(0, (int)pa.x, 0x104, 0xf, 0xf, true);
            const unsigned ny = (unsigned)__builtin_amdgcn_update_dpp(0, (int)pa.y, 0x104, 0xf, 0xf, true);
            if (l >= 40) pa.x = ts;
            if (l < 48 && !(l & 4))
                st16(ws, (unsigned)((l >> 3) * 64 + (l & 3) * 16), SB + L_PM + (unsigned)s * PM_SZ + (unsigned)h * 768u + (unsigned)w * 384u,
                     (u32x4){pa.x, pa.y, nx, ny});
            TR(6);
        }
    }
}
DI void role_ms(const Params& p, rsrc_t ws, int L, int h, unsigned char* lds) {
    if (__builtin_amdgcn_readfirstlane(threadIdx.x >> 8) == 0) role_ms_t<0>(p, ws, L, h, lds); else role_ms_t<1>(p, ws, L, h, lds);
}

DI void role_v(const Params& p, rsrc_t ws, int L, int v, unsigned char* lds) {
    constexpr unsigned MS_OFF = 0, Y_OFF = 7168, OP_OFF = 15360, W2_OFF = 23552, W3_OFF = W2_OFF + 65536;
    const int tid = threadIdx.x, w = __builtin_amdgcn_readfirstlane(tid >> 6), l = tid & 63, g = l >> 4, r16 = l & 15;
    const unsigned SB = OFF_ST + (unsigned)L * L_TOTAL;
    const u32x4* fb = (const u32x4*)(p.ws + OFF_FB);
    auto wunit = [&](int tile, int kk, int gg, int n16) -> u32x4 {
        const int wc = tile * 16 + n16, hh = wc / 28, c = 28 * v + wc % 28;
        return fb[(((c >> 4) * 16 + hh) * 14 + kk) * 64 + gg * 16 + (c & 15)];
    };
#pragma unroll
    for (int k = 0; k < 8; ++k) sts16(lds, W2_OFF + (unsigned)((w * 8 + k) * 1024 + l * 16), wunit(16 + w, 6 + k, g, r16));
#pragma unroll
    for (int k = 0; k < 7; ++k) {
        const int u = tid + 512 * k;
        sts16(lds, W3_OFF + u * 16u, wunit(24 + u / 896, (u / 64) % 14, (u >> 4) & 3, u & 15));
    }
    bf16x16 wr[2][7], wr2[3];
#pragma unroll
    for (int t = 0; t < 2; ++t)
#pragma unroll
        for (int ks = 0; ks < 7; ++ks) wr[t][ks] = cat16(wunit(2 * w + t, 2 * ks, g, r16), wunit(2 * w + t, 2 * ks + 1, g, r16));
#pragma unroll
    for (int ks = 0; ks < 3; ++ks) wr2[ks] = cat16(wunit(16 + w, 2 * ks, g, r16), wunit(16 + w, 2 * ks + 1, g, r16));
    const int sidx = (r16 & 8) ? 0xEEEE : 0x4444;
    const int ha = 2 * w;
    unsigned uoff[2]; unsigned yoff[2];
#pragma unroll
    for (int t = 0; t < 2; ++t) {
        int cl = 16 * t + r16; if (cl > 27) cl = 27;
        const int c = 28 * v + cl;
        uoff[t] = (unsigned)(((c >> 4) * 16 * 64 + g * 16 + (c & 15)) * 16 + ha * 1024);
        yoff[t] = Y_OFF + (unsigned)(((ha + (g >> 1)) * 28 + cl) * 16 + (g & 1) * 8);
    }
    const unsigned ptok = (unsigned)(ha * 768 + ((r16 >> 2) & 1) * 384 + g * 64 + (r16 & 3) * 16);
    const unsigned pmem = (unsigned)((ha + (g >> 1)) * 768 + ((r16 >> 2) & 1) * 384 + 256 + (r16 >> 3) * 64 + (r16 & 3) * 16 + (g & 1) * 8);
    const unsigned ys0 = Y_OFF + (unsigned)(((2 * w + (g >> 1)) * 16 + r16) * 16 + (g & 1) * 8);
    const unsigned ys1 = Y_OFF + (unsigned)((((g >> 1) ? 24 + w : 16 + w) * 16 + r16) * 16 + (g & 1) * 8);
    const unsigned w2 = W2_OFF + (unsigned)(w * 8192 + l * 16), w3 = W3_OFF + (unsigned)((w & 3) * 14336 + l * 16);
    const int ct0 = (28 * v) >> 4, ct1 = (28 * v + 27) >> 4;
    const unsigned fU = (unsigned)L * FLG_L + FLG_U + (unsigned)((min(ct0 + min(l, 2), ct1) * 8 + w) * 128);
    const unsigned fY = (unsigned)L * FLG_L + FLG_Y + (unsigned)((v * 8 + w) * 128);
    unsigned haveU = 0;
    const unsigned mu = (unsigned)tid * 16u;
    unsigned prm = PRE_V_MEM, pre = PRE_V_PM;
    unsigned t0 = now() - PRE_V_PM;

    for (int s = 0; s < NSEG; ++s) {
        TR(0); TW(0);
        {
            const unsigned src = SB + L_MEM + (unsigned)s * MEM_SZ;
            const bool waited = pace(t0, prm);
            const unsigned r0 = stamp();
            TR(1); TW(1);
            u32x4 m;
            unsigned spins = 0;
            for (;;) {
                m = ld16(ws, mu, src);
                if (none_sent(mx4(m))) break;
                __builtin_amdgcn_s_sleep(LAZY_GAP);
                if (!spin_ok(spins, ws)) return;
            }
            mem_stage<true, false>(m, lds, MS_OFF, 0, tid);
            prm = steer(prm, waited, r0, __builtin_amdgcn_readlane(m.x, 28), __builtin_amdgcn_readlane(m.x, 60), TGT_MEM + 100);
        }
        TR(2); TW(2);
        __syncthreads();
        TW(3);
        STAT(0, st_a); STAT(1, st_b); STAT(2, st_c); STAT(3, st_d); STAT(4, st_e); STAT(5, st_f); STAT(6, st_g); STAT(7, st_h);
        if (v == 0 && w == 7) post_flag(ws, (unsigned)L * FLG_L + FLG_M, (unsigned)s + 1u, l);
        u32x4 uf[2][2];
        {
            const int su = ISO(s < NSEG - 1 ? s : NSEG - 2);
            if (!wait_flag(ws, fU, (unsigned)su + 1u, haveU)) return;
            const unsigned ub = SB + L_U + (unsigned)su * U_SZ;
#pragma unroll
            for (int t = 0; t < 2; ++t) { uf[0][t] = ld16(ws, uoff[t], ub); uf[1][t] = ld16(ws, uoff[t], ub + 1024u); }
        }
        asm volatile("" ::: "memory");
        f32x4 acc[4];
#pragma unroll
        for (int t = 0; t < 4; ++t) acc[t] = ZERO4;
#pragma unroll
        for (int ks = 0; ks < 7; ++ks) {
            const bf16x8 ma = as8(lds16(lds, MS_OFF + ks * 1024u + l * 16u));
#pragma unroll
            for (int t = 0; t < 2; ++t) acc[t] = smfma(ma, wr[t][ks], acc[t], sidx);
            if (ks < 3) acc[2] = smfma(ma, wr2[ks], acc[2], sidx);
            else acc[2] = smfma(ma, cat16(lds16(lds, w2 + (ks - 3) * 2048u), lds16(lds, w2 + (ks - 3) * 2048u + 1024u)), acc[2], sidx);
        }
        if (w < 4) {
            asm volatile("" ::: "memory");
#pragma unroll
            for (int ks = 0; ks < 7; ++ks)
                acc[3] = smfma(as8(lds16(lds, MS_OFF + ks * 1024u + l * 16u)),
                               cat16(lds16(lds, w3 + ks * 2048u), lds16(lds, w3 + ks * 2048u + 1024u)), acc[3], sidx);
        }
        TW(4);
        {
            f32x4 y0, y1;
#pragma unroll
            for (int r = 0; r < 4; ++r) { y0[r] = half_add(acc[0][r], acc[1][r]); y1[r] = half_add(acc[2][r], acc[3][r]); }
            sts8(lds, ys0, pk4(y0)); sts8(lds, ys1, pk4(y1));
        }
        __syncthreads();
        const u32x2 yf0 = lds8(lds, yoff[0]), yf1 = lds8(lds, yoff[1]);
        st16(ws, (unsigned)tid * 16u, SB + L_Y + (unsigned)s * Y_SZ + (unsigned)v * 8192u, (u32x4){yf0.x, yf0.y, yf1.x, yf1.y});
        asm volatile("" ::: "memory");
        if (s == NSEG - 1) { drain(); post_flag(ws, fY, (unsigned)NSEG, l); break; }
        u32x4 ya = {yf0.x, yf0.y, 0u, 0u}, yb = {yf1.x, yf1.y, 0u, 0u};
        TR(3); TW(5);
        {
            const unsigned pbase = SB + L_PM + (unsigned)s * PM_SZ;
            const bool waited = pace(t0, pre);
            const unsigned r0 = stamp();
            u32x4 pa, pb; u32x2 qm;
            unsigned spins = 0;
            for (;;) {
                pa = ld16(ws, ptok, pbase); pb = ld16(ws, ptok, pbase + 768u); qm = ld8(ws, pmem, pbase);
                if (none_sent(max(max(mx4(pa), mx4(pb)), max(qm.x, qm.y)))) break;
                if (HOT_GAP) __builtin_amdgcn_s_sleep(HOT_GAP);
                if (!spin_ok(spins, ws)) return;
            }
            TR(4); TW(6);
            const u32x4 q4 = {qm.x, qm.y, 0u, 0u};
            f32x4 o0 = mfma(as8(uf[0][0]), as8(pa), ZERO4), o1 = mfma(as8(uf[0][1]), as8(pa), ZERO4);
            o0 = mfma(as8(uf[1][0]), as8(pb), o0); o1 = mfma(as8(uf[1][1]), as8(pb), o1);
            o0 = mfma(as8(ya), as8(q4), o0); o1 = mfma(as8(yb), as8(q4), o1);
            if (r16 < 8) {
                stsf4(lds, OP_OFF + (unsigned)((w * 64 + g * 8 + r16) * 16), o0);
                stsf4(lds, OP_OFF + (unsigned)((w * 64 + 32 + g * 8 + r16) * 16), o1);
            }
            pre = steer(pre, waited, r0, later(__builtin_amdgcn_readlane(qm.x, 8), __builtin_amdgcn_readlane(qm.x, 12)),
                        later(__builtin_amdgcn_readlane(qm.x, 40), __builtin_amdgcn_readlane(qm.x, 44)), TGT_PM);
        }
        TW(7);
        __syncthreads();
        TR(5); TW(8);
        t0 = now();
        if (w == 0) {
            const unsigned ts = stamp();
            const int lc = l < 56 ? l : 55;
            f32x4 o[8];
#pragma unroll
            for (int ww = 0; ww < 8; ++ww) o[ww] = ldsf4(lds, OP_OFF + (unsigned)((ww * 64 + lc) * 16));
            const f32x4 r = ((o[0] + o[1]) + (o[2] + o[3])) + ((o[4] + o[5]) + (o[6] + o[7]));
            u32x2 pr = pk4s(r);
            const unsigned nx = (unsigned)__builtin_amdgcn_update_dpp(0, (int)pr.x, 0xF5, 0xf, 0xf, true);
            const unsigned ny = (unsigned)__builtin_amdgcn_update_dpp(0, (int)pr.y, 0xF5, 0xf, 0xf, true);
            if (l >= 56) pr.x = ts;
            if (!(l & 1)) st16(ws, (unsigned)l * 8u, SB + L_MEM + (unsigned)(s + 1) * MEM_SZ + (unsigned)v * 512u, (u32x4){pr.x, pr.y, nx, ny});
            TR(6); TW(9);
        }
        post_flag(ws, fY, (unsigned)s + 1u, l);
    }
}

DI void role_ts(const Params& p, rsrc_t ws, int L, int y, unsigned char* lds) {
    constexpr unsigned XN_OFF = 0, MK_OFF = 28672, SP_OFF = 35840, W_OFF = 76800;
    const int h = y >> 1;
    const int tid = threadIdx.x, w = __builtin_amdgcn_readfirstlane(tid >> 6), l = tid & 63, g = l >> 4, r16 = l & 15;
    const unsigned SB = OFF_ST + (unsigned)L * L_TOTAL;
    const u32x4* fa = (const u32x4*)(p.ws + OFF_FA + (size_t)h * 28 * 14 * 1024);
    bf16x8 wr[3][14];
#pragma unroll
    for (int t = 0; t < 3; ++t)
#pragma unroll
        for (int kk = 0; kk < 14; ++kk) wr[t][kk] = as8(fa[((3 * w + t) * 14 + kk) * 64 + l]);
#pragma unroll
    for (int k = 0; k < 7; ++k) sts16(lds, W_OFF + (tid + 512 * k) * 16u, fa[24 * 14 * 64 + tid + 512 * k]);
    const int nt3 = 24 + (w & 3);
    const unsigned xw = W_OFF + (unsigned)(w & 3) * 14336u + l * 16u, xx = XN_OFF + (unsigned)(w >> 2) * 1024u + l * 16u;

    unsigned haveM = 0;
    for (int s = y & 1; s < NSEG; s += 2) {
        TR(0);
        if (!fetch_xn(ws, SB + L_XN + (unsigned)s * XN_SZ, lds, XN_OFF, tid, w, tid)) return;
        __syncthreads();
        TR(1);
        u32x2 tf[2][4];
#pragma unroll
        for (int mt = 0; mt < 2; ++mt) {
            f32x4 acc[3];
#pragma unroll
            for (int t = 0; t < 3; ++t) acc[t] = ZERO4;
#pragma unroll
            for (int kk = 0; kk < 14; ++kk) {
                const bf16x8 xb = as8(lds16(lds, XN_OFF + (unsigned)((kk * 2 + mt) * 1024 + l * 16)));
#pragma unroll
                for (int t = 0; t < 3; ++t) acc[t] = mfma(wr[t][kk], xb, acc[t]);
            }
#pragma unroll
            for (int t = 0; t < 3; ++t) tf[mt][t] = pk4(acc[t]);
        }
        {
            f32x4 acc = ZERO4;
#pragma unroll
            for (int kk = 0; kk < 14; ++kk)
                acc = mfma(as8(lds16(lds, xw + (unsigned)kk * 1024u)), as8(lds16(lds, xx + (unsigned)kk * 2048u)), acc);
            const u32x2 t3 = pk4(acc), z = {0u, 0u};
            tf[0][3] = w < 4 ? t3 : z; tf[1][3] = w < 4 ? z : t3;
        }
        f32x4 sa[5];
#pragma unroll
        for (int k = 0; k < 5; ++k) sa[k] = ZERO4;
#pragma unroll
        for (int pr = 0; pr < 2; ++pr) {
            const int na = 3 * w + 2 * pr, nb = pr == 0 ? 3 * w + 1 : nt3;
            const bf16x8 k0 = cat8(xn_key(lds, XN_OFF, na, 0, g, r16), xn_key(lds, XN_OFF, nb, 0, g, r16));
            const bf16x8 k1 = cat8(xn_key(lds, XN_OFF, na, 1, g, r16), xn_key(lds, XN_OFF, nb, 1, g, r16));
            const bf16x8 t0 = cat8(tf[0][2 * pr], tf[0][2 * pr + 1]), t1 = cat8(tf[1][2 * pr], tf[1][2 * pr + 1]);
            sa[2] = mfma(k0, t0, sa[2]); sa[3] = mfma(k0, t1, sa[3]); sa[4] = mfma(k1, t1, sa[4]);
        }
#pragma unroll
        for (int k = 2; k < 5; ++k) stsf4(lds, SP_OFF + (unsigned)(((w * 5 + k) * 64 + l) * 16), sa[k]);
        TR(2);
        if (!wait_flag(ws, (unsigned)L * FLG_L + FLG_M, (unsigned)s + 1u, haveM)) return;
        if (!fetch_mem<false, true, false>(ws, SB + L_MEM + (unsigned)s * MEM_SZ, lds, 0, MK_OFF, tid, w)) return;
        __syncthreads();
        TR(3);
#pragma unroll
        for (int pr = 0; pr < 2; ++pr) {
            const int na = 3 * w + 2 * pr, nb = pr == 0 ? 3 * w + 1 : nt3;
            const bf16x8 km = cat8(mem_key(lds, MK_OFF, na, g, r16), mem_key(lds, MK_OFF, nb, g, r16));
            sa[0] = mfma(km, cat8(tf[0][2 * pr], tf[0][2 * pr + 1]), sa[0]);
            sa[1] = mfma(km, cat8(tf[1][2 * pr], tf[1][2 * pr + 1]), sa[1]);
        }
#pragma unroll
        for (int k = 0; k < 2; ++k) stsf4(lds, SP_OFF + (unsigned)(((w * 5 + k) * 64 + l) * 16), sa[k]);
        __syncthreads();
        {
            const int row = tid >> 4, q = tid & 15, mt = row >> 4, i = row & 15;
            const int qq = q < 10 ? q : 9;
            const int kt = qq < 2 ? 0 : (qq < 6 ? 1 : 2), gq = qq < 2 ? qq : ((qq - 2) & 3);
            const bool live = (q < 10) && !(kt == 2 && mt == 0);
            const int tile = kt == 0 ? mt : (kt == 1 ? 2 + mt : 4);
            f32x4 sv = ZERO4;
#pragma unroll
            for (int ww = 0; ww < 8; ++ww) sv += ldsf4(lds, SP_OFF + (unsigned)(((ww * 5 + tile) * 64 + gq * 16 + i) * 16));
            bool lv[4];
#pragma unroll
            for (int r = 0; r < 4; ++r) {
                const int key = (kt - 1) * 16 + 4 * gq + r;
                lv[r] = live && (kt == 0 || key <= row);
                sv[r] = lv[r] ? sv[r] : -3.0e38f;
            }
            const float mx = row16_max(fmaxf(fmaxf(sv[0], sv[1]), fmaxf(sv[2], sv[3])));
            f32x4 e;
#pragma unroll
            for (int r = 0; r < 4; ++r) e[r] = lv[r] ? __builtin_amdgcn_exp2f((sv[r] - mx) * LOG2E) : 0.f;
            const float inv = __builtin_amdgcn_rcpf(row16_sum((e[0] + e[1]) + (e[2] + e[3])));
            e *= inv;
            const unsigned po = (unsigned)h * 2560u + (unsigned)mt * 1280u +
                                (kt == 0 ? 1024u + (unsigned)(gq * 16 + i) * 8u : (unsigned)(gq * 16 + i) * 16u + (unsigned)(kt - 1) * 8u);
            if (q < 10) st8(ws, po, SB + L_PT + (unsigned)s * PT_SZ, pk4s(e));
            TR(6);
        }
    }
}

DI void role_vu(const Params& p, rsrc_t ws, int L, int ct, unsigned char* lds) {
    const int tid = threadIdx.x, w = __builtin_amdgcn_readfirstlane(tid >> 6), l = tid & 63;
    const unsigned SB = OFF_ST + (unsigned)L * L_TOTAL;
    const u32x4* fb = (const u32x4*)(p.ws + OFF_FB + (size_t)ct * 16 * 14 * 1024);
    bf16x8 wr[2][14];
#pragma unroll
    for (int t = 0; t < 2; ++t)
#pragma unroll
        for (int kk = 0; kk < 14; ++kk) wr[t][kk] = as8(fb[((2 * w + t) * 14 + kk) * 64 + l]);
    const unsigned fU = (unsigned)L * FLG_L + FLG_U + (unsigned)((ct * 8 + w) * 128);
    const unsigned uo = (unsigned)((ct * 16 + 2 * w) * 1024 + l * 16);
    XnRegs x;
    xn_issue(ws, SB + L_XN, tid, x);
    int posted = 0;
    for (int s = 0; s < NSEG; ++s) {
        TR(0);
        const unsigned xo = (unsigned)(s & 1) * XN_SZ, src = SB + L_XN + (unsigned)s * XN_SZ;
        for (unsigned spins = 0; !xn_ok(x);) {
            if (posted < s) { drain(); post_flag(ws, fU, (unsigned)s, l); posted = s; }
            if (!retry(ws, src + (unsigned)w * 1024u, spins)) return;
            xn_issue(ws, src, tid, x);
        }
        xn_stage(x, lds, xo, tid);
        __syncthreads();
        TR(1);
        asm volatile("" ::: "memory");
        xn_issue(ws, s + 1 < NSEG ? src + XN_SZ : src, tid, x);
        asm volatile("" ::: "memory");
        f32x4 acc[2][2];
#pragma unroll
        for (int t = 0; t < 2; ++t) { acc[t][0] = ZERO4; acc[t][1] = ZERO4; }
#pragma unroll
        for (int kk = 0; kk < 14; ++kk)
#pragma unroll
            for (int mt = 0; mt < 2; ++mt) {
                const bf16x8 xa = as8(lds16(lds, xo + (unsigned)((kk * 2 + mt) * 1024 + l * 16)));
#pragma unroll
                for (int t = 0; t < 2; ++t) acc[t][mt] = mfma(xa, wr[t][kk], acc[t][mt]);
            }
        if (posted < s) {
            asm volatile("s_waitcnt vmcnt(4)" ::: "memory");
            post_flag(ws, fU, (unsigned)s, l); posted = s;
        }
#pragma unroll
        for (int t = 0; t < 2; ++t) {
            const u32x2 a = pk4(acc[t][0]), b = pk4(acc[t][1]);
            st16(ws, uo, SB + L_U + (unsigned)s * U_SZ + t * 1024u, (u32x4){a.x, a.y, b.x, b.y});
        }
        TR(6);
    }
    drain();
    post_flag(ws, fU, (unsigned)NSEG, l);
}

DI void role_vo(const Params& p, rsrc_t ws, int L, int v, unsigned char* lds) {
    const int tid = threadIdx.x, w = __builtin_amdgcn_readfirstlane(tid >> 6), l = tid & 63, g = l >> 4, r16 = l & 15;
    const unsigned SB = OFF_ST + (unsigned)L * L_TOTAL;
    const int ha = 2 * w;
    unsigned uoff[2];
#pragma unroll
    for (int t = 0; t < 2; ++t) {
        int cl = 16 * t + r16; if (cl > 27) cl = 27;
        const int c = 28 * v + cl;
        uoff[t] = (unsigned)(((c >> 4) * 16 * 64 + g * 16 + (c & 15)) * 16 + ha * 1024);
    }
    const unsigned ptok = (unsigned)(ha * 2560 + l * 16), pmem = (unsigned)((ha + (g >> 1)) * 2560 + 1024 + ((g & 1) * 16 + r16) * 8);
    const int ct0 = (28 * v) >> 4, ct1 = (28 * v + 27) >> 4;
    const unsigned fU = (unsigned)L * FLG_L + FLG_U + (unsigned)((min(ct0 + min(l, 2), ct1) * 8 + w) * 128);
    const unsigned fY = (unsigned)L * FLG_L + FLG_Y + (unsigned)((v * 8 + w) * 128);
    unsigned haveU = 0, haveY = 0;
    const int row = (tid >> 3) & 31, qd = tid & 7, qc = qd < 7 ? qd : 6;
    const int Q = 7 * v + qc;
    const unsigned xmo = (unsigned)((Q >> 2) * 1024 + row * 32 + (Q & 3) * 8);
    const unsigned rdo = (unsigned)((((qc >> 2) * 2 + (row >> 4)) * 64 + (qc & 3) * 16 + (row & 15)) * 16);
    for (int s = 0; s < NSEG; ++s) {
        TR(0);
        if (!wait_flag(ws, fU, (unsigned)s + 1u, haveU)) return;
        u32x4 uf[2][2];
        {
            const unsigned ub = SB + L_U + (unsigned)s * U_SZ;
#pragma unroll
            for (int t = 0; t < 2; ++t) { uf[0][t] = ld16(ws, uoff[t], ub); uf[1][t] = ld16(ws, uoff[t], ub + 1024u); }
        }
        if (!wait_flag(ws, fY, (unsigned)s + 1u, haveY)) return;
        const u32x4 yf = ld16(ws, (unsigned)tid * 16u, SB + L_Y + (unsigned)s * Y_SZ + (unsigned)v * 8192u);
        TR(1);
        u32x4 pa[2], pb[2]; u32x2 qm[2], xr;
        {
            const unsigned pbase = SB + L_PT + (unsigned)s * PT_SZ;
            for (unsigned spins = 0;;) {
#pragma unroll
                for (int mt = 0; mt < 2; ++mt) {
                    pa[mt] = ld16(ws, ptok, pbase + mt * 1280u); pb[mt] = ld16(ws, ptok, pbase + 2560u + mt * 1280u);
                    qm[mt] = ld8(ws, pmem, pbase + mt * 1280u);
                }
                xr = ld8(ws, xmo, SB + L_XMID + (unsigned)s * XN_SZ);
                bool ok = ok2(xr);
#pragma unroll
                for (int mt = 0; mt < 2; ++mt) ok = ok & ok4(pa[mt]) & ok4(pb[mt]) & ok2(qm[mt]);
                if (__all(ok)) break;
                if (!retry(ws, pbase + (unsigned)ha * 2560u, spins)) return;
            }
        }
        TR(2);
        const unsigned ob = (unsigned)(s & 1) * 32768u;
#pragma unroll
        for (int t = 0; t < 2; ++t)
#pragma unroll
            for (int mt = 0; mt < 2; ++mt) {
                f32x4 o = ZERO4;
                o = mfma(as8(uf[0][t]), as8(pa[mt]), o);
                o = mfma(as8(uf[1][t]), as8(pb[mt]), o);
                o = mfma16(t ? (u32x2){yf.z, yf.w} : (u32x2){yf.x, yf.y}, qm[mt], o);
                stsf4(lds, ob + (unsigned)((((w * 2 + t) * 2 + mt) * 64 + l) * 16), o);
            }
        __syncthreads();
        if (w < 4) {
            f32x4 o = ZERO4;
#pragma unroll
            for (int ww = 0; ww < 8; ++ww) o += ldsf4(lds, ob + rdo + (unsigned)ww * 4096u);
            f32x4 r;
            r[0] = bflo(xr.x) + bfr(o[0]); r[1] = bfhi(xr.x) + bfr(o[1]); r[2] = bflo(xr.y) + bfr(o[2]); r[3] = bfhi(xr.y) + bfr(o[3]);
            if (L == 0) {
                u32x2 pv = pk4s(r);
                const float a0 = bflo(pv.x), a1 = bfhi(pv.x), a2 = bflo(pv.y), a3 = bfhi(pv.y);
                float s1 = qd < 7 ? (a0 + a1) + (a2 + a3) : 0.f;
                float s2 = qd < 7 ? __builtin_fmaf(a0, a0, a1 * a1) + __builtin_fmaf(a2, a2, a3 * a3) : 0.f;
                s1 += dpp<0xB1>(s1); s2 += dpp<0xB1>(s2);
                s1 += dpp<0x4E>(s1); s2 += dpp<0x4E>(s2);
                s1 += dpp<0x141>(s1); s2 += dpp<0x141>(s2);
                if (qd == 7) pv = (u32x2){nosent(ubits(s1)), nosent(ubits(s2))};
                const unsigned nx = (unsigned)__builtin_amdgcn_update_dpp(0, (int)pv.x, 0xF5, 0xf, 0xf, true);
                const unsigned ny = (unsigned)__builtin_amdgcn_update_dpp(0, (int)pv.y, 0xF5, 0xf, 0xf, true);
                if (!(qd & 1)) st16(ws, (unsigned)((row >> 4) * 1024 + (qd >> 1) * 256 + (row & 15) * 16),
                                    OFF_ST + L_TOTAL + L_XI + (unsigned)s * XI_SZ + (unsigned)v * 2048u, (u32x4){pv.x, pv.y, nx, ny});
            } else if (qd < 7) {
                *(u32x2*)(p.out + (size_t)(s * 32 + row) * DM + Q * 4) = pk4(r);
            }
            TR(6);
        }
    }
}

DI void role_wo(const Params& p, rsrc_t ws, int L, int y, unsigned char* lds) {
    constexpr unsigned OI_OFF = 0, ROWB = 912, RED_OFF = OI_OFF + 16 * ROWB, W_OFF = 16384;
    const int mt = y & 1;
    const int tid = threadIdx.x, w = __builtin_amdgcn_readfirstlane(tid >> 6), l = tid & 63, g = l >> 4, r16 = l & 15;
    const unsigned SB = OFF_ST + (unsigned)L * L_TOTAL;
    const u32x4* fw = (const u32x4*)(p.ws + OFF_FW);
    bf16x8 wr[3][14];
#pragma unroll
    for (int t = 0; t < 3; ++t)
#pragma unroll
        for (int kk = 0; kk < 14; ++kk) wr[t][kk] = as8(fw[((3 * w + t) * 14 + kk) * 64 + l]);
#pragma unroll
    for (int k = 0; k < 7; ++k) sts16(lds, W_OFF + (tid + 512 * k) * 16u, fw[24 * 14 * 64 + tid + 512 * k]);
    const int ntile = w < 4 ? 4 : 3;
    unsigned xio[4];
#pragma unroll
    for (int t = 0; t < 4; ++t) {
        const int nt = t < 3 ? 3 * w + t : 24 + (w & 3), Q = 4 * nt + g;
        xio[t] = (unsigned)((Q / 7) * 2048 + mt * 1024 + ((Q % 7) >> 1) * 256 + r16 * 16 + ((Q % 7) & 1) * 8);
    }
    unsigned xno[2], xmo[2];
#pragma unroll
    for (int pr = 0; pr < 2; ++pr) {
        const int t = 2 * pr + (g & 1), nt = t < 3 ? 3 * w + t : 24 + (w & 3);
        xno[pr] = (unsigned)(((nt >> 1) * 2 + mt) * 1024 + (nt & 1) * 512 + ((g >> 1) * 16 + r16) * 16);
        xmo[pr] = (unsigned)(nt * 1024 + (mt * 16 + r16) * 32 + (g >> 1) * 16);
    }
    const unsigned oo = (unsigned)(w * 2048 + mt * 1024 + l * 16);
    for (int s = (y >> 1) & 1; s < NSEG; s += 2) {
        TR(0);
        u32x4 ov[2]; u32x2 xr[4];
        {
            const unsigned ob = SB + L_O + (unsigned)s * O_SZ, xb = SB + L_XI + (unsigned)s * XI_SZ;
            for (unsigned spins = 0;;) {
                ov[0] = ld16(ws, oo, ob); ov[1] = ld16(ws, oo, ob + 16384u);
                bool ok = ok4(ov[0]) & ok4(ov[1]);
#pragma unroll
                for (int t = 0; t < 4; ++t) { xr[t] = ld8(ws, xio[t], xb); ok = ok & ok2(xr[t]); }
                if (__all(ok)) break;
                if (!retry(ws, ob + (unsigned)(w * 2048 + mt * 1024), spins)) return;
            }
        }
        TR(1);
#pragma unroll
        for (int k = 0; k < 2; ++k) {
            const int hh = w + 8 * k, row = (l >> 1) & 15, u4 = (l >> 5) * 2 + (l & 1);
            const unsigned d = OI_OFF + (unsigned)(row * ROWB + hh * 56 + u4 * 16);
            sts8(lds, d, (u32x2){ov[k].x, ov[k].y});
            if (u4 != 3) sts8(lds, d + 8, (u32x2){ov[k].z, ov[k].w});
        }
        __syncthreads();
        f32x4 acc[4];
#pragma unroll
        for (int t = 0; t < 4; ++t) acc[t] = ZERO4;
#pragma unroll
        for (int kk = 0; kk < 14; ++kk) {
            const bf16x8 ob = as8(lds16(lds, OI_OFF + (unsigned)(r16 * ROWB + kk * 64 + g * 16)));
#pragma unroll
            for (int t = 0; t < 3; ++t) acc[t] = mfma(wr[t][kk], ob, acc[t]);
        }
        if (w < 4) {
            asm volatile("" ::: "memory");
#pragma unroll
            for (int kk = 0; kk < 14; ++kk)
                acc[3] = mfma(as8(lds16(lds, W_OFF + (unsigned)((w * 14 + kk) * 1024 + l * 16))),
                              as8(lds16(lds, OI_OFF + (unsigned)(r16 * ROWB + kk * 64 + g * 16))), acc[3]);
        }
        TR(2);
        f32x4 xm[4]; float sum = 0.f, sq = 0.f;
#pragma unroll
        for (int t = 0; t < 4; ++t) {
            xm[t][0] = bfr(bflo(xr[t].x) + bfr(acc[t][0])); xm[t][1] = bfr(bfhi(xr[t].x) + bfr(acc[t][1]));
            xm[t][2] = bfr(bflo(xr[t].y) + bfr(acc[t][2])); xm[t][3] = bfr(bfhi(xr[t].y) + bfr(acc[t][3]));
            if (t < ntile) {
                sum += (xm[t][0] + xm[t][1]) + (xm[t][2] + xm[t][3]);
                sq += __builtin_fmaf(xm[t][0], xm[t][0], xm[t][1] * xm[t][1]) + __builtin_fmaf(xm[t][2], xm[t][2], xm[t][3] * xm[t][3]);
            }
        }
        sum = xg_sum(sum); sq = xg_sum(sq);
        if (l < 16) sts8(lds, RED_OFF + (unsigned)(l * 64 + w * 8), (u32x2){ubits(sum), ubits(sq)});
        __syncthreads();
        float mean, rstd;
        {
            const f32x4 r0 = ldsf4(lds, RED_OFF + r16 * 64u), r1 = ldsf4(lds, RED_OFF + r16 * 64u + 16u),
                        r2 = ldsf4(lds, RED_OFF + r16 * 64u + 32u), r3 = ldsf4(lds, RED_OFF + r16 * 64u + 48u);
            const f32x4 rs = (r0 + r1) + (r2 + r3);
            mean = (rs[0] + rs[2]) * (1.0f / DM);
            rstd = __builtin_amdgcn_rsqf(fmaxf(__builtin_fmaf(-mean, mean, (rs[1] + rs[3]) * (1.0f / DM)), 0.f) + 1e-5f);
        }
#pragma unroll
        for (int pass = 0; pass < 2; ++pass)
#pragma unroll
            for (int pr = 0; pr < 2; ++pr) {
                const u32x2 a = pk4s(pass == 0 ? (xm[2 * pr] - mean) * rstd : xm[2 * pr]);
                const u32x2 b = pk4s(pass == 0 ? (xm[2 * pr + 1] - mean) * rstd : xm[2 * pr + 1]);
                const auto qx = __builtin_amdgcn_permlane16_swap(a.x, b.x, false, false), qy = __builtin_amdgcn_permlane16_swap(a.y, b.y, false, false);
                const u32x4 u = {qx[0], qy[0], qx[1], qy[1]};
                if (pr == 0 || w < 4 || !(g & 1))
                    st16(ws, pass == 0 ? xno[pr] : xmo[pr], SB + (pass == 0 ? L_XN : L_XMID) + (unsigned)s * XN_SZ, u);
            }
        TR(6);
    }
}

DI void role_at(const Params& p, rsrc_t ws, int L, int h, unsigned char* lds) {
    constexpr unsigned MGW = 2048 + 128;
    constexpr unsigned RB = 80, VRB = 1040, MG_OFF = 0, Q_OFF = 8 * MGW, ST_OFF = Q_OFF + 32 * RB, XA_OFF = ST_OFF + 256,
                       K_OFF = XA_OFF + 32768, V_OFF = K_OFF + 512 * RB;
    const int tid = threadIdx.x, w = __builtin_amdgcn_readfirstlane(tid >> 6), l = tid & 63, g = l >> 4, r16 = l & 15;
    const unsigned SB = OFF_ST + (unsigned)L * L_TOTAL;
    const u32x4* fq = (const u32x4*)(p.ws + OFF_FQ + (size_t)h * 6 * 16 * 1024);
    const int wt = w < 4 ? w : 4 + (w & 1);
    bf16x8 wr[16];
#pragma unroll
    for (int kk = 0; kk < 16; ++kk) wr[kk] = as8(fq[(wt * 16 + kk) * 64 + l]);
    f32x4 wsum = ZERO4;
    {
        const bf16x8 one = as8((u32x4){0x3F803F80u, 0x3F803F80u, 0x3F803F80u, 0x3F803F80u});
#pragma unroll
        for (int kk = 0; kk < 16; ++kk) wsum = w < 4 ? mfma(wr[kk], one, wsum) : mfma(one, wr[kk], wsum);
    }
    unsigned xo[4];
#pragma unroll
    for (int k = 0; k < 4; ++k) {
        const int pc = (w - 2) * 16 + k * 4 + g;
        xo[k] = (unsigned)(w < 2 ? ((4 * k + g) * 2 + w) * 1024 + 768 + r16 * 16 : (pc / 3) * 1024 + (pc % 3) * 256 + r16 * 16);
    }
    const int qt = w & 1, kq = w >> 1;
    const float scl = 0.18898223650461362f * LOG2E;
    u32x4 xv[4];
#pragma unroll
    for (int k = 0; k < 4; ++k) xv[k] = ld16(ws, xo[k], SB + L_XI);
    bool okx = none_sent(max(max(mx4(xv[0]), mx4(xv[1])), max(mx4(xv[2]), mx4(xv[3]))));

    for (int s = 0; s < NSEG; ++s) {
        TR(0);
        const unsigned xb = SB + L_XI + (unsigned)s * XI_SZ;
        for (unsigned spins = 0; !okx;) {
            if (!retry(ws, xb + (unsigned)w * 4096u, spins)) return;
#pragma unroll
            for (int k = 0; k < 4; ++k) xv[k] = ld16(ws, xo[k], xb);
            okx = none_sent(max(max(mx4(xv[0]), mx4(xv[1])), max(mx4(xv[2]), mx4(xv[3]))));
        }
        TR(1);
        if (w < 2) {
            const float s1 = xg_sum((fbits(xv[0].z) + fbits(xv[1].z)) + (fbits(xv[2].z) + fbits(xv[3].z)));
            const float s2 = xg_sum((fbits(xv[0].w) + fbits(xv[1].w)) + (fbits(xv[2].w) + fbits(xv[3].w)));
            const float mean = s1 * (1.0f / DM);
            const float var = fmaxf(__builtin_fmaf(-mean, mean, s2 * (1.0f / DM)), 0.f);
            const float rstd = __builtin_amdgcn_rsqf(var + 1e-5f);
            if (l < 16) sts8(lds, ST_OFF + (unsigned)(w * 16 + l) * 8u, (u32x2){ubits(rstd), ubits(-mean * rstd)});
#pragma unroll
            for (int k = 0; k < 4; ++k) { xv[k].z = 0u; xv[k].w = 0u; }
        }
#pragma unroll
        for (int k = 0; k < 4; ++k) sts16(lds, XA_OFF + xo[k], xv[k]);
        {
            const unsigned xn = s + 1 < NSEG ? xb + XI_SZ : xb;
#pragma unroll
            for (int k = 0; k < 4; ++k) xv[k] = ld16(ws, xo[k], xn);
        }
        __syncthreads();
        TR(2);
        const int slot = s & 15;
        if (w < 4) {
            f32x4 acc[2];
            acc[0] = ZERO4; acc[1] = ZERO4;
            const u32x2 st0 = lds8(lds, ST_OFF + (unsigned)r16 * 8u), st1 = lds8(lds, ST_OFF + 128u + (unsigned)r16 * 8u);
#pragma unroll
            for (int kk = 0; kk < 16; ++kk)
#pragma unroll
                for (int mt = 0; mt < 2; ++mt)
                    acc[mt] = mfma(wr[kk], as8(lds16(lds, XA_OFF + (unsigned)((kk * 2 + mt) * 1024 + l * 16))), acc[mt]);
            const unsigned base = w < 2 ? Q_OFF : K_OFF + (unsigned)slot * 32u * RB;
#pragma unroll
            for (int r = 0; r < 4; ++r) {
                acc[0][r] = __builtin_fmaf(acc[0][r], fbits(st0.x), wsum[r] * fbits(st0.y));
                acc[1][r] = __builtin_fmaf(acc[1][r], fbits(st1.x), wsum[r] * fbits(st1.y));
            }
#pragma unroll
            for (int mt = 0; mt < 2; ++mt) sts8(lds, base + (unsigned)((mt * 16 + r16) * RB + (w & 1) * 32 + g * 8), pk4(acc[mt]));
        } else {
            const unsigned mt = (unsigned)(w >> 1) & 1u;
            const f32x4 sa = ldsf4(lds, ST_OFF + mt * 128u + (unsigned)g * 32u), sb = ldsf4(lds, ST_OFF + mt * 128u + (unsigned)g * 32u + 16u);
            f32x4 acc = ZERO4;
#pragma unroll
            for (int kk = 0; kk < 16; ++kk)
                acc = mfma(as8(lds16(lds, XA_OFF + mt * 1024u + (unsigned)(kk * 2048 + l * 16))), wr[kk], acc);
            acc[0] = __builtin_fmaf(acc[0], sa[0], wsum[0] * sa[1]); acc[1] = __builtin_fmaf(acc[1], sa[2], wsum[0] * sa[3]);
            acc[2] = __builtin_fmaf(acc[2], sb[0], wsum[0] * sb[1]); acc[3] = __builtin_fmaf(acc[3], sb[2], wsum[0] * sb[3]);
            sts8(lds, V_OFF + (unsigned)(((w & 1) * 16 + r16) * VRB + slot * 64 + g * 16) + mt * 8u, pk4(acc));
        }
        __syncthreads();
        TR(3);
        {
            constexpr float NEG = -3.0e38f;
            const bf16x8 qf = as8(lds16(lds, Q_OFF + (unsigned)((qt * 16 + r16) * RB + g * 16)));
            int qd = qt * 16 + r16 - 4 * g;
            asm volatile("" : "+v"(qd));
            const unsigned kl = K_OFF + (unsigned)(r16 * RB + g * 16), vl = V_OFF + (unsigned)(r16 * VRB + g * 16);
            f32x4 sc[2][2];
#pragma unroll
            for (int jj = 0; jj < 2; ++jj) {
                const int j = kq + 4 * jj;
                const unsigned sl = (unsigned)(s - j) & 15u;
#pragma unroll
                for (int kt = 0; kt < 2; ++kt) {
                    sc[jj][kt] = mfma(as8(lds16(lds, kl + (sl * 32u + kt * 16u) * RB)), qf, ZERO4);
                    if (j > s) sc[jj][kt] = (f32x4){NEG, NEG, NEG, NEG};
                }
            }
            if (kq == 0) {
                const unsigned sl = (unsigned)(s - 8) & 15u;
#pragma unroll
                for (int kt = 0; kt < 2; ++kt) {
                    f32x4 c = mfma(as8(lds16(lds, kl + (sl * 32u + kt * 16u) * RB)), qf, ZERO4);
                    if (s < 8) c = (f32x4){NEG, NEG, NEG, NEG};
#pragma unroll
                    for (int r = 0; r < 4; ++r) sc[0][kt][r] = qd >= kt * 16 + r ? sc[0][kt][r] : c[r];
                }
            }
            float mx = NEG;
#pragma unroll
            for (int jj = 0; jj < 2; ++jj)
#pragma unroll
                for (int kt = 0; kt < 2; ++kt)
                    mx = fmaxf(fmaxf(mx, fmaxf(sc[jj][kt][0], sc[jj][kt][1])), fmaxf(sc[jj][kt][2], sc[jj][kt][3]));
            mx = xg_max(mx) * scl;
            float sum = 0.f;
            f32x4 o[2];
            o[0] = ZERO4; o[1] = ZERO4;
#pragma unroll
            for (int jj = 0; jj < 2; ++jj) {
                const int j = kq + 4 * jj;
                const unsigned sl = (unsigned)(s - j) & 15u;
                f32x4 e[2];
#pragma unroll
                for (int kt = 0; kt < 2; ++kt) {
#pragma unroll
                    for (int r = 0; r < 4; ++r) e[kt][r] = __builtin_amdgcn_exp2f(__builtin_fmaf(sc[jj][kt][r], scl, -mx));
                    sum += (e[kt][0] + e[kt][1]) + (e[kt][2] + e[kt][3]);
                }
                if (jj == 0 && kq == 0) {
                    f32x4 e8[2];
#pragma unroll
                    for (int kt = 0; kt < 2; ++kt)
#pragma unroll
                        for (int r = 0; r < 4; ++r) {
                            const bool near_ = qd >= kt * 16 + r;
                            e8[kt][r] = near_ ? 0.f : e[kt][r]; e[kt][r] = near_ ? e[kt][r] : 0.f;
                        }
                    if (s >= 8) {
                        const unsigned s8 = (unsigned)(s - 8) & 15u;
                        const bf16x8 pf = cat8(pk4(e8[0]), pk4(e8[1]));
#pragma unroll
                        for (int dt = 0; dt < 2; ++dt) o[dt] = mfma(as8(lds16(lds, vl + dt * 16u * VRB + s8 * 64u)), pf, o[dt]);
                    }
                }
                if (j <= s) {
                    const bf16x8 pf = cat8(pk4(e[0]), pk4(e[1]));
#pragma unroll
                    for (int dt = 0; dt < 2; ++dt) o[dt] = mfma(as8(lds16(lds, vl + dt * 16u * VRB + sl * 64u)), pf, o[dt]);
                }
            }
            sum = kq > s ? 0.f : xg_sum(sum);
            const unsigned mb = MG_OFF + (unsigned)w * MGW;
            stsf4(lds, mb + l * 16u, o[0]); stsf4(lds, mb + 1024u + l * 16u, o[1]);
            if (l < 16) sts8(lds, mb + 2048u + l * 8u, (u32x2){ubits(mx), ubits(sum)});
        }
        okx = none_sent(max(max(mx4(xv[0]), mx4(xv[1])), max(mx4(xv[2]), mx4(xv[3]))));
        __syncthreads();
        TR(4);
        if (w < 4) {
            const int dt = w >> 1;
            float m4[4], l4[4]; float M = -3.0e38f;
            f32x4 pv[4];
#pragma unroll
            for (int k = 0; k < 4; ++k) {
                const u32x2 ml = lds8(lds, MG_OFF + (unsigned)(2 * k + qt) * MGW + 2048u + r16 * 8u);
                pv[k] = ldsf4(lds, MG_OFF + (unsigned)(2 * k + qt) * MGW + (unsigned)(dt * 1024 + l * 16));
                m4[k] = fbits(ml.x); l4[k] = fbits(ml.y); M = fmaxf(M, m4[k]);
            }
            float lsum = 0.f; f32x4 o = ZERO4;
#pragma unroll
            for (int k = 0; k < 4; ++k) {
                const float f = __builtin_amdgcn_exp2f(m4[k] - M);
                lsum += l4[k] * f;
                o += pv[k] * f;
            }
            o *= __builtin_amdgcn_rcpf(lsum);
            if (dt == 1 && g == 3) o = ZERO4;
            const u32x2 po = pk4s(o);
            const auto qx = __builtin_amdgcn_permlane16_swap(po.x, po.x, false, false), qy = __builtin_amdgcn_permlane16_swap(po.y, po.y, false, false);
            if (!(g & 1)) st16(ws, (unsigned)((qt * 2 + dt) * 512 + r16 * 32 + g * 8), SB + L_O + (unsigned)s * O_SZ + (unsigned)h * 2048u,
                               (u32x4){qx[0], qy[0], qx[1], qy[1]});
            TR(6);
        }
    }
}

__global__ void __launch_bounds__(NT, 2) k_main(Params p) {
    extern __shared__ __attribute__((aligned(16))) unsigned char lds[];
    const rsrc_t ws = __builtin_amdgcn_make_buffer_rsrc(p.ws, 0, WS_NEED, 0x00020000);
    const int b = blockIdx.x, x = b & 7, y = b >> 3;
    if (x < 2) {
        if (y < 16) role_ms(p, ws, x, y, lds); else role_v(p, ws, x, y - 16, lds);
    } else {
        const int L = x >= 5 ? 1 : 0, z = x - 2 - 3 * L;
        if (z == 0) role_ts(p, ws, L, y, lds);
        else if (z == 1) { if (y < 28) role_vu(p, ws, L, y, lds); else role_wo(p, ws, L, y - 28, lds); }
        else { if (y < 16) role_at(p, ws, L, y, lds); else role_vo(p, ws, L, y - 16, lds); }
    }
}

__global__ void __launch_bounds__(NT, 2) k_pro(Params p) {
    extern __shared__ __attribute__((aligned(16))) unsigned char lds[];
    const int b = blockIdx.x, tid = threadIdx.x, w = __builtin_amdgcn_readfirstlane(tid >> 6), l = tid & 63, g = l >> 4, r16 = l & 15;
    if (b < 224) {
        const bool isB = b >= 112;
        const int bb = isB ? b - 112 : b, h = bb / 7, nb = bb % 7;
        constexpr unsigned RS = 528;
        if (isB) {
#pragma unroll
            for (int k = 0; k < 4; ++k) {
                const int u = tid + 512 * k, e = u >> 3, c8 = u & 7;
                const u32x4 v = *(const u32x4*)(p.Wo + ((size_t)h * 256 + e) * DM + nb * 64 + c8 * 8);
#pragma unroll
                for (int j = 0; j < 4; ++j) {
                    *(uint16_t*)(lds + (c8 * 8 + 2 * j) * RS + e * 2) = (uint16_t)(v[j] & 0xFFFFu);
                    *(uint16_t*)(lds + (c8 * 8 + 2 * j + 1) * RS + e * 2) = (uint16_t)(v[j] >> 16);
                }
            }
        } else {
#pragma unroll
            for (int k = 0; k < 4; ++k) {
                const int u = tid + 512 * k, n = u >> 5, e8 = u & 31;
                sts16(lds, n * RS + e8 * 16, *(const u32x4*)(p.Wk + ((size_t)(nb * 64 + n) * 16 + h) * 256 + e8 * 8));
            }
        }
        __syncthreads();
        if (w == 7) {
            const u32x4 sent = {SENT, SENT, SENT, SENT};
            constexpr unsigned KB0 = L_O / 1024, KBM = L_MEM / 1024, KBL = L_ARMED / 1024;
            for (unsigned k = KB0 + (unsigned)b; k < 2 * KBL; k += 224u) {
                const unsigned L = k >= KBL ? 1u : 0u, kb = k - L * KBL;
                if (kb >= KBM && kb < KBM + MEM_SZ / 1024) continue;
                *(u32x4*)(p.ws + OFF_ST + (size_t)L * L_TOTAL + (size_t)kb * 1024 + l * 16) = sent;
            }
        }
        if (w < 7) {
            const uint16_t* Wd = isB ? p.Wv : p.Wq;
            f32x4 acc[4][4];
#pragma unroll
            for (int i = 0; i < 4; ++i)
#pragma unroll
                for (int j = 0; j < 4; ++j) acc[i][j] = (f32x4){0.f, 0.f, 0.f, 0.f};
#pragma unroll
            for (int half = 0; half < 2; ++half) {
                u32x4 af[4][4];
#pragma unroll
                for (int ke = 0; ke < 4; ++ke)
#pragma unroll
                    for (int i = 0; i < 4; ++i)
                        af[ke][i] = *(const u32x4*)(Wd + ((size_t)(w * 64 + i * 16 + r16) * 16 + h) * 256 + (half * 4 + ke) * 32 + g * 8);
#pragma unroll
                for (int ke = 0; ke < 4; ++ke)
#pragma unroll
                    for (int j = 0; j < 4; ++j) {
                        const bf16x8 bfr_ = as8(lds16(lds, (unsigned)((j * 16 + r16) * RS + (half * 4 + ke) * 64 + g * 16)));
#pragma unroll
                        for (int i = 0; i < 4; ++i) acc[i][j] = mfma(as8(af[ke][i]), bfr_, acc[i][j]);
                    }
            }
            const float sc = isB ? 1.0f : 0.0625f;
#pragma unroll
            for (int i = 0; i < 4; ++i)
#pragma unroll
                for (int j = 0; j < 4; ++j) {
                    const int nt = nb * 4 + j, d0 = w * 64 + i * 16 + 4 * g, kk = d0 >> 5;
                    const size_t tile = isB ? (size_t)(nt * 16 + h) : (size_t)(h * 28 + nt);
                    unsigned char* dst = p.ws + (isB ? OFF_FB : OFF_FA) + (tile * 14 + kk) * 1024 + (((d0 >> 3) & 3) * 16 + r16) * 16 + (d0 & 7) * 2;
                    *(u32x2*)dst = pk4(acc[i][j] * sc);
                }
        }
        return;
    }
    const int gt = (b - 224) * NT + tid;
    for (int u = gt; u < 16 * 6 * 16 * 64; u += 32 * NT) {
        const int ln = u & 63, kk = (u >> 6) & 15, t = (u >> 10) % 6, h = u / 6144;
        const int cl = (t & 1) * 16 + (ln & 15), d0 = kk * 28 + (ln >> 4) * 8;
        u32x4 o = {0u, 0u, 0u, 0u};
        if (cl < 28) {
            const uint16_t* src = p.wqkv + (size_t)d0 * 1344 + (t >> 1) * 448 + h * 28 + cl;
#pragma unroll
            for (int j = 0; j < 4; ++j)
                if (j < 2 || (ln >> 4) != 3) o[j] = (unsigned)src[(2 * j) * 1344] | ((unsigned)src[(2 * j + 1) * 1344] << 16);
        }
        *(u32x4*)(p.ws + OFF_FQ + (size_t)u * 16) = o;
    }
    for (int u = gt; u < 28 * 14 * 64; u += 32 * NT) {
        const int ln = u & 63, kk = (u >> 6) % 14, t = u / 896;
        const uint16_t* src = p.wo + (size_t)(kk * 32 + (ln >> 4) * 8) * DM + t * 16 + (ln & 15);
        u32x4 o;
#pragma unroll
        for (int j = 0; j < 4; ++j) o[j] = (unsigned)src[(2 * j) * DM] | ((unsigned)src[(2 * j + 1) * DM] << 16);
        *(u32x4*)(p.ws + OFF_FW + (size_t)u * 16) = o;
    }
    for (int u = gt; u < NSEG * 2048; u += 32 * NT) {
        const int u4 = (u >> 4) & 3, row = ((u >> 6) & 1) * 16 + (u & 15), v = (u >> 7) & 15, s = u >> 11;
        const uint16_t* src = p.x + (size_t)(s * 32 + row) * DM + v * 28 + u4 * 8;
        const u32x2 a = *(const u32x2*)src;
        u32x2 c;
        if (u4 != 3) c = *(const u32x2*)(src + 4);
        else {
            float s1 = 0.f, s2 = 0.f;
#pragma unroll
            for (int q = 0; q < 7; ++q) {
                const u32x2 e = *(const u32x2*)(src - 24 + q * 4);
                const float a0 = bflo(e.x), a1 = bfhi(e.x), a2 = bflo(e.y), a3 = bfhi(e.y);
                s1 += (a0 + a1) + (a2 + a3);
                s2 += __builtin_fmaf(a0, a0, a1 * a1) + __builtin_fmaf(a2, a2, a3 * a3);
            }
            c = (u32x2){ubits(s1), ubits(s2)};
        }
        *(u32x4*)(p.ws + OFF_ST + L_XI + (size_t)u * 16) = (u32x4){nosent(a.x), nosent(a.y), nosent(c.x), nosent(c.y)};
    }
    if (gt < 2 * 16 * 7 * 8) {
        const int L = gt >= 896 ? 1 : 0, e = gt - L * 896, i = e & 7, Q = e >> 3, v = Q / 7, q = Q % 7;
        u32x2 m = *(const u32x2*)(p.mem0 + i * DM + Q * 4);
        m.x = nosent(m.x); m.y = nosent(m.y);
        *(u32x2*)(p.ws + OFF_ST + (size_t)L * L_TOTAL + L_MEM + v * 512 + q * 64 + i * 8) = m;
        if (q == 6) *(u32x2*)(p.ws + OFF_ST + (size_t)L * L_TOTAL + L_MEM + v * 512 + 448 + i * 8) = (u32x2){0u, 0u};
    }
    for (unsigned u = gt; u < (OFF_FLG + SZ_FLG) / 16; u += 32 * NT) *(u32x4*)(p.ws + (size_t)u * 16) = (u32x4){0u, 0u, 0u, 0u};
}

}

extern "C" void kernel_launch(void* const* d_in, const int* in_sizes, int n_in, void* d_out, int out_size, void* d_ws, size_t ws_size,
                              hipStream_t stream) {
    Params p;
    p.x = (const uint16_t*)d_in[0]; p.wqkv = (const uint16_t*)d_in[1]; p.wo = (const uint16_t*)d_in[2];
    p.mem0 = (const uint16_t*)d_in[4]; p.Wq = (const uint16_t*)d_in[5]; p.Wk = (const uint16_t*)d_in[6];
    p.Wv = (const uint16_t*)d_in[7]; p.Wo = (const uint16_t*)d_in[8];
    p.out = (uint16_t*)d_out; p.ws = (unsigned char*)d_ws;
    constexpr int LDS_PRO = 64 * 528;
    (void)hipFuncSetAttribute((const void*)k_main, hipFuncAttributeMaxDynamicSharedMemorySize, LDS_MAIN);
    hipLaunchKernelGGL(k_pro, dim3(256), dim3(NT), LDS_PRO, stream, p);
    hipLaunchKernelGGL(k_main, dim3(256), dim3(NT), LDS_MAIN, stream, p);
}
```

```cpp
#include <hip/hip_runtime.h>
#include <hip/hip_bf16.h>
#include <stdint.h>

namespace {

typedef __bf16 bf16x2 __attribute__((ext_vector_type(2)));
typedef __bf16 bf16x8 __attribute__((ext_vector_type(8)));
typedef __bf16 bf16x16 __attribute__((ext_vector_type(16)));
typedef short s16x4 __attribute__((ext_vector_type(4)));
typedef float f32x4 __attribute__((ext_vector_type(4)));
typedef unsigned u32x2 __attribute__((ext_vector_type(2)));
typedef unsigned u32x4 __attribute__((ext_vector_type(4)));
typedef unsigned u32x8 __attribute__((ext_vector_type(8)));
#define LAS __attribute__((address_space(3)))
#define DI __device__ __forceinline__

constexpr int DM = 448, NSEG = 64, NT = 512;
constexpr unsigned SENT = 0xFFFFFFFFu;
constexpr unsigned SPIN_CAP = 1u << 17;

constexpr unsigned OFF_CTRL = 0;
constexpr unsigned OFF_FLG = 4096, FLG_U = 0, FLG_Y = 28u * 8 * 128, FLG_M = FLG_Y + 16u * 8 * 128, FLG_L = 49152, SZ_FLG = 2 * FLG_L;
constexpr unsigned OFF_FA = OFF_FLG + SZ_FLG, SZ_FA = 16u * 28 * 14 * 1024;
constexpr unsigned OFF_FB = OFF_FA + SZ_FA;
constexpr unsigned OFF_FQ = OFF_FB + SZ_FA, SZ_FQ = 16u * 6 * 16 * 1024;
constexpr unsigned OFF_FW = OFF_FQ + SZ_FQ, SZ_FW = 28u * 14 * 1024;
constexpr unsigned OFF_ST = OFF_FW + SZ_FW;
#define MREP 0
#define PREP 0
constexpr unsigned XI_SZ = 32768, XN_SZ = 28672, O_SZ = 32768, MEM_SZ = MREP ? 16384 : 8192, PM_SZ = PREP ? 24576 : 12288, PT_SZ = 40960, U_SZ = 458752, Y_SZ = 131072;
constexpr unsigned L_XI = 0, L_O = L_XI + 64u * XI_SZ, L_XN = L_O + 64u * O_SZ, L_XMID = L_XN + 64u * XN_SZ,
                   L_MEM = L_XMID + 64u * XN_SZ, L_PM = L_MEM + 64u * MEM_SZ, L_PT = L_PM + 64u * PM_SZ, L_ARMED = L_PT + 64u * PT_SZ,
                   L_U = L_ARMED, L_Y = L_U + 64u * U_SZ, L_TOTAL = L_Y + 64u * Y_SZ;
constexpr unsigned WS_NEED = OFF_ST + 2u * L_TOTAL;
#define ARM_K 6u
constexpr int LDS_MAIN = 160 * 1024;

struct Params {
    const uint16_t* x;
    const uint16_t* wqkv;
    const uint16_t* wo;
    const uint16_t* mem0;
    const uint16_t* Wq;
    const uint16_t* Wk;
    const uint16_t* Wv;
    const uint16_t* Wo;
    uint16_t* out;
    unsigned char* ws;
};

typedef __amdgpu_buffer_rsrc_t rsrc_t;
DI u32x4 ld16(rsrc_t r, unsigned vo, unsigned so) { return __builtin_amdgcn_raw_buffer_load_b128(r, vo, so, 16); }
DI u32x2 ld8(rsrc_t r, unsigned vo, unsigned so) { return __builtin_amdgcn_raw_buffer_load_b64(r, vo, so, 16); }
DI unsigned ld4(rsrc_t r, unsigned vo, unsigned so) { return __builtin_amdgcn_raw_buffer_load_b32(r, vo, so, 16); }
DI void st16(rsrc_t r, unsigned vo, unsigned so, u32x4 v) {
    __builtin_amdgcn_raw_buffer_store_b128(v, r, vo, so, 16);
    asm volatile("s_nop 1" : "+v"(v));
}
#define AUXL 16
#define AUXS 16
DI u32x4 ld16c(rsrc_t r, unsigned vo, unsigned so) { return __builtin_amdgcn_raw_buffer_load_b128(r, vo, so, AUXL); }
DI u32x2 ld8c(rsrc_t r, unsigned vo, unsigned so) { return __builtin_amdgcn_raw_buffer_load_b64(r, vo, so, AUXL); }
DI void st16c(rsrc_t r, unsigned vo, unsigned so, u32x4 v) {
    __builtin_amdgcn_raw_buffer_store_b128(v, r, vo, so, AUXS);
    asm volatile("s_nop 1" : "+v"(v));
}
DI void st8(rsrc_t r, unsigned vo, unsigned so, u32x2 v) { __builtin_amdgcn_raw_buffer_store_b64(v, r, vo, so, 16); }
DI void st4(rsrc_t r, unsigned vo, unsigned so, unsigned v) { __builtin_amdgcn_raw_buffer_store_b32(v, r, vo, so, 16); }
#define SENT4 ((u32x4){SENT, SENT, SENT, SENT})
DI bool ok4(u32x4 v) { return (v.x != SENT) & (v.y != SENT) & (v.z != SENT) & (v.w != SENT); }
DI bool ok2(u32x2 v) { return (v.x != SENT) & (v.y != SENT); }
DI void drain() { asm volatile("s_waitcnt vmcnt(0)" ::: "memory"); }
DI unsigned uni(unsigned v) { return (unsigned)__builtin_amdgcn_readfirstlane((int)v); }

#define PROBE_GAP 4
DI bool spin_ok(unsigned& spins, rsrc_t ws) {
    asm volatile("" ::: "memory");
    ++spins;
    if ((spins & 63u) == 0u) {
        if (spins > SPIN_CAP) { st4(ws, 0, OFF_CTRL, 1u); return false; }
        if (uni(ld4(ws, 0, OFF_CTRL)) != 0u) return false;
    }
    return true;
}
DI bool probe(rsrc_t ws, unsigned so, unsigned& spins) {
    for (;;) {
        __builtin_amdgcn_s_sleep(PROBE_GAP);
        if (uni(ld4(ws, 0, so)) != SENT) return true;
        if (!spin_ok(spins, ws)) return false;
    }
}
DI bool retry(rsrc_t ws, unsigned so, unsigned& spins) {
    if (spins == 0u) { spins = 1u; return probe(ws, so, spins); }
    return spin_ok(spins, ws);
}
DI bool wait_flag(rsrc_t ws, unsigned vo, unsigned need, unsigned& have) {
    for (unsigned spins = 0; have < need;) {
        const unsigned v = ld4(ws, vo, OFF_FLG);
        const unsigned a = __builtin_amdgcn_readlane(v, 0), b = __builtin_amdgcn_readlane(v, 1), c = __builtin_amdgcn_readlane(v, 2);
        have = min(a, min(b, c));
        if (have >= need) break;
        __builtin_amdgcn_s_sleep(2);
        if (!spin_ok(spins, ws)) return false;
    }
    asm volatile("" ::: "memory");
    return true;
}
constexpr unsigned NOWHERE = 0x80000000u;
DI void post_flag(rsrc_t ws, unsigned so, unsigned v, int l) { st4(ws, l == 0 ? 0u : NOWHERE, OFF_FLG + so, v); }

DI unsigned pk2(float a, float b) { bf16x2 v = {(__bf16)a, (__bf16)b}; return __builtin_bit_cast(unsigned, v); }
DI unsigned nosent(unsigned x) { return x < 0xFFFFFFFEu ? x : 0xFFFFFFFEu; }
DI u32x2 pk4(f32x4 v) { return (u32x2){pk2(v[0], v[1]), pk2(v[2], v[3])}; }
DI u32x2 pk4s(f32x4 v) { return (u32x2){nosent(pk2(v[0], v[1])), nosent(pk2(v[2], v[3]))}; }
DI float bflo(unsigned x) { return __builtin_bit_cast(float, x << 16); }
DI float bfhi(unsigned x) { return __builtin_bit_cast(float, x & 0xFFFF0000u); }
DI float bfr(float x) { return (float)(__bf16)x; }
DI bf16x8 as8(u32x4 v) { return __builtin_bit_cast(bf16x8, v); }
DI bf16x8 cat8(u32x2 a, u32x2 b) { return as8((u32x4){a.x, a.y, b.x, b.y}); }
DI bf16x16 cat16(u32x4 a, u32x4 b) { return __builtin_bit_cast(bf16x16, (u32x8){a.x, a.y, a.z, a.w, b.x, b.y, b.z, b.w}); }
DI f32x4 mfma(bf16x8 a, bf16x8 b, f32x4 c) { return __builtin_amdgcn_mfma_f32_16x16x32_bf16(a, b, c, 0, 0, 0); }
DI f32x4 mfma16(u32x2 a, u32x2 b, f32x4 c) {
    return mfma(as8((u32x4){a.x, a.y, 0u, 0u}), as8((u32x4){b.x, b.y, 0u, 0u}), c);
}
DI f32x4 smfma(bf16x8 a, bf16x16 b, f32x4 c, int idx) { return __builtin_amdgcn_smfmac_f32_16x16x64_bf16(a, b, c, idx, 0, 0); }
template <int CTRL> DI float dpp(float v) {
    return __builtin_bit_cast(float, __builtin_amdgcn_update_dpp(0, __builtin_bit_cast(int, v), CTRL, 0xf, 0xf, true));
}
DI float row16_max(float v) {
    v = fmaxf(v, dpp<0x128>(v)); v = fmaxf(v, dpp<0x124>(v)); v = fmaxf(v, dpp<0x122>(v)); v = fmaxf(v, dpp<0x121>(v)); return v;
}
DI float row16_sum(float v) {
    v += dpp<0x128>(v); v += dpp<0x124>(v); v += dpp<0x122>(v); v += dpp<0x121>(v); return v;
}
DI float fbits(unsigned v) { return __builtin_bit_cast(float, v); }
DI unsigned ubits(float v) { return __builtin_bit_cast(unsigned, v); }
DI float half_add(float a, float b) {
    const auto q = __builtin_amdgcn_permlane32_swap(ubits(a), ubits(b), false, false);
    return fbits(q[0]) + fbits(q[1]);
}
DI float xg_max(float x) {
    auto q = __builtin_amdgcn_permlane32_swap(ubits(x), ubits(x), false, false);
    x = fmaxf(fbits(q[0]), fbits(q[1]));
    q = __builtin_amdgcn_permlane16_swap(ubits(x), ubits(x), false, false);
    return fmaxf(fbits(q[0]), fbits(q[1]));
}
DI float xg_sum(float x) {
    auto q = __builtin_amdgcn_permlane32_swap(ubits(x), ubits(x), false, false);
    x = fbits(q[0]) + fbits(q[1]);
    q = __builtin_amdgcn_permlane16_swap(ubits(x), ubits(x), false, false);
    return fbits(q[0]) + fbits(q[1]);
}
DI u32x4 lds16(const unsigned char* lds, unsigned off) { return *(const u32x4*)(lds + off); }
DI u32x2 lds8(const unsigned char* lds, unsigned off) { return *(const u32x2*)(lds + off); }
DI f32x4 ldsf4(const unsigned char* lds, unsigned off) { return *(const f32x4*)(lds + off); }
DI void sts16(unsigned char* lds, unsigned off, u32x4 v) { *(u32x4*)(lds + off) = v; }
DI void sts8(unsigned char* lds, unsigned off, u32x2 v) { *(u32x2*)(lds + off) = v; }
DI void sts4(unsigned char* lds, unsigned off, unsigned v) { *(unsigned*)(lds + off) = v; }
DI void stsf4(unsigned char* lds, unsigned off, f32x4 v) { *(f32x4*)(lds + off) = v; }
DI u32x2 lds_tr(unsigned char* lds, unsigned off) {
    return __builtin_bit_cast(u32x2, __builtin_amdgcn_ds_read_tr16_b64_v4i16((LAS s16x4*)(lds + off)));
}
constexpr float LOG2E = 1.4426950408889634f;
#define ZERO4 ((f32x4){0.f, 0.f, 0.f, 0.f})

#define TR(ev) do {} while (0)
#define ISO(s) (s)
#define STAT(i, val) do {} while (0)
#define TW(ev) do {} while (0)
struct XnRegs { u32x4 v[4]; };
DI void xn_issue(rsrc_t ws, unsigned src, int tid, XnRegs& x) {
    x.v[0] = ld16(ws, tid * 16u, src); x.v[1] = ld16(ws, tid * 16u, src + 8192u); x.v[2] = ld16(ws, tid * 16u, src + 16384u);
    x.v[3] = ld16(ws, (tid & 255) * 16u, src + 24576u);
}
DI bool xn_ok(const XnRegs& x) { return __all(ok4(x.v[0]) & ok4(x.v[1]) & ok4(x.v[2]) & ok4(x.v[3])); }
DI void xn_stage(const XnRegs& x, unsigned char* lds, unsigned xn_off, int tid) {
    sts16(lds, xn_off + tid * 16u, x.v[0]); sts16(lds, xn_off + tid * 16u + 8192u, x.v[1]); sts16(lds, xn_off + tid * 16u + 16384u, x.v[2]);
    if (tid < 256) sts16(lds, xn_off + (1536u + tid) * 16u, x.v[3]);
}
DI bool fetch_xn(rsrc_t ws, unsigned src, unsigned char* lds, unsigned xn_off, int tid, int w, unsigned& spins) {
    XnRegs x;
    for (spins = 0;;) {
        xn_issue(ws, src, tid, x);
        if (xn_ok(x)) break;
        if (!retry(ws, src + (unsigned)w * 1024u, spins)) return false;
    }
    xn_stage(x, lds, xn_off, tid);
    return true;
}
DI bool fetch_xn6(rsrc_t ws, unsigned src, unsigned char* lds, unsigned xn_off, int t6, int w) {
    u32x4 x[5];
    for (unsigned spins = 0;;) {
#pragma unroll
        for (int k = 0; k < 4; ++k) x[k] = ld16(ws, t6 * 16u, src + k * 6144u);
        x[4] = ld16(ws, (t6 & 255) * 16u, src + 24576u);
        if (__all(ok4(x[0]) & ok4(x[1]) & ok4(x[2]) & ok4(x[3]) & ok4(x[4]))) break;
        if (!retry(ws, src + (unsigned)w * 1024u, spins)) return false;
    }
#pragma unroll
    for (int k = 0; k < 4; ++k) sts16(lds, xn_off + t6 * 16u + k * 6144u, x[k]);
    if (t6 < 256) sts16(lds, xn_off + 24576u + t6 * 16u, x[4]);
    return true;
}
template <bool WS_, bool WK_, bool HOT>
DI bool fetch_mem(rsrc_t ws, unsigned src, unsigned char* lds, unsigned ms_off, unsigned mk_off, int tid, int w) {
    const int v = tid >> 5, q0 = (tid >> 2) & 7, rp = tid & 3;
    const int q = q0 == 7 ? 6 : q0;
    const unsigned off = (unsigned)(v * 512 + q * 64 + rp * 16);
    u32x4 m;
    for (unsigned spins = 0;;) {
        m = ld16(ws, off, src);
        if (__all(ok4(m))) break;
        if (HOT) { if (!spin_ok(spins, ws)) return false; }
        else if (!retry(ws, src + (unsigned)w * 1024u, spins)) return false;
    }
    if (q0 != 7) {
        const int Q = 7 * v + q;
        if (WK_) sts16(lds, mk_off + (unsigned)(Q * 64 + rp * 16), m);
        if (WS_) {
            const unsigned base = ms_off + (unsigned)((Q >> 4) * 1024 + ((Q >> 2) & 3) * 256 + (Q & 3) * 4 + rp * 32);
            sts4(lds, base, m.x); sts4(lds, base + 128, m.y); sts4(lds, base + 16, m.z); sts4(lds, base + 144, m.w);
        }
    }
    return true;
}
#define PRE_MS_MEM 4100
#define PRE_V_PM 5600
#define PRE_V_MEM 900
#define GAP 5
#define XN_DELAY 9
#define SCHED() __builtin_amdgcn_sched_barrier(0)
DI unsigned now() { return (unsigned)__builtin_amdgcn_s_memtime(); }
DI void sleep_until(unsigned t0, unsigned pre) { while (now() - t0 < pre) __builtin_amdgcn_s_sleep(1); }
#define VEXTRA 0
#define TGT_MEM 800
#define TGT_PM 1000
#define BK_PM 0
#define BK_MEM 0
#define GATE0 2000u
#define GATE1 3000u
#define HINT_GAP 2
#define ST_FX 0
#define ST_FCAP 256
#define ST_UP 256
#define ST_DN 2
#define ST_DCAP 256
#define ST_SHIFT 2
DI unsigned stamp() { return ((unsigned)__builtin_amdgcn_s_memrealtime() & 0x3FFFFFFEu) | 0x40000000u; }
#define PEN 1000
constexpr unsigned MARK = 0x80000000u;
DI unsigned later(unsigned a, unsigned b) { return (int)((a - b) << 2) > 0 ? a : b; }
DI unsigned wg_latest(const unsigned char* lds, unsigned off) {
    const u32x4 a = lds16(lds, off), b = lds16(lds, off + 16u);
    const unsigned m = uni((a.x | a.y | a.z | a.w | b.x | b.y | b.z | b.w) & MARK);
    return uni(later(later(later(a.x, a.y), later(a.z, a.w)), later(later(b.x, b.y), later(b.z, b.w)))) | m;
}
DI bool pace(unsigned t0, unsigned pre) {
    if (now() - t0 >= pre) return false;
    while (now() - t0 < pre) __builtin_amdgcn_s_sleep(1);
    return true;
}
DI unsigned steer(unsigned pre, bool waited, unsigned fails, unsigned r0, unsigned ta, unsigned tb, int target) {
    if (ta == 0u || tb == 0u) return pre;
    const int sa = (int)((r0 - ta) << 2) >> 2, sb = (int)((r0 - tb) << 2) >> 2;
    const int err = target - max(min(min(sa, sb), 1000), -1000) * 23;
    if (err < 0 && !waited) return pre;
    const int d = fails ? min(max(err, 0) + ST_FX, ST_FCAP) : err > 0 ? min(err >> ST_SHIFT, ST_UP) : max(err >> ST_DN, -ST_DCAP);
    return uni((unsigned)max((int)pre + d, 0));
}
#define W3P 4
#define ULATE 0
#define XN6 0
#define WGM 0
#define WGV 0
#define TADD 50
#define TSHINT 4
#define VO_GAP 2
#define AT_GAP 4
#define HOT_GAP 0
#define LAZY_GAP 4
DI unsigned mx4(u32x4 v) { return max(max(v.x, v.y), max(v.z, v.w)); }
DI bool none_sent(unsigned t) { return __builtin_amdgcn_ballot_w64(t == SENT) == 0ull; }
template <bool WS_, bool WK_>
DI void mem_stage(u32x4 m, unsigned char* lds, unsigned ms_off, unsigned mk_off, int tid) {
    const int v = tid >> 5, q = (tid >> 2) & 7, rp = tid & 3;
    if (q != 7) {
        const int Q = 7 * v + q;
        if (WK_) sts16(lds, mk_off + (unsigned)(Q * 64 + rp * 16), m);
        if (WS_) {
            const unsigned base = ms_off + (unsigned)((Q >> 4) * 1024 + ((Q >> 2) & 3) * 256 + (Q & 3) * 4 + rp * 32);
            sts4(lds, base, m.x); sts4(lds, base + 128, m.y); sts4(lds, base + 16, m.z); sts4(lds, base + 144, m.w);
        }
    }
}
DI unsigned mem_unit(int tid) { const int q0 = (tid >> 2) & 7; return (unsigned)((tid >> 5) * 512 + (q0 == 7 ? 6 : q0) * 64 + (tid & 3) * 16); }
DI u32x2 xn_key(const unsigned char* lds, unsigned xn_off, int nt, int mt, int g, int r16) {
    return lds8(lds, xn_off + (unsigned)(((nt >> 1) * 2 + mt) * 1024 + ((2 * (nt & 1) + (g >> 1)) * 16 + r16) * 16 + (g & 1) * 8));
}
DI u32x2 mem_key(const unsigned char* lds, unsigned mk_off, int nt, int g, int r16) {
    return lds8(lds, mk_off + (unsigned)(((4 * nt + g) * 8 + (r16 & 7)) * 8));
}

template <int HI>
DI void ms_scores(unsigned char* lds, const bf16x16 (&wr)[3][7], const u32x4 (&wsh)[4], const u32x4 (&kt0)[2], unsigned xw, int sidx,
                  unsigned mab, unsigned ttw, unsigned ttr, unsigned kx1, unsigned km0, unsigned km1, f32x4 (&sa)[3]) {
    constexpr int NS = HI ? 3 : 4, NB = 7 - NS, K0 = HI ? 4 : 0, K1 = HI ? 0 : 4;
    constexpr int ORD[7] = {K0, K0 + 1, K0 + 2, HI ? 0 : 3, HI ? 1 : 4, HI ? 2 : 5, HI ? 3 : 6};
    bf16x8 ma[7];
#define MA(i) ma[ORD[i]] = as8(lds16(lds, mab + ORD[i] * 1024u))
#define T3(k) do { acc[0] = smfma(ma[k], wr[0][k], acc[0], sidx); acc[1] = smfma(ma[k], wr[1][k], acc[1], sidx); \
                   acc[2] = smfma(ma[k], wr[2][k], acc[2], sidx); } while (0)
    MA(0); MA(1); MA(2);
    f32x4 acc[4];
#pragma unroll
    for (int t = 0; t < 4; ++t) acc[t] = ZERO4;
    u32x4 w2[2], w3[2], kt1[2], kq0, kq1;
    SCHED();
    acc[3] = smfma(ma[K0], cat16(wsh[0], wsh[1]), acc[3], sidx);
    SCHED();
    w2[0] = lds16(lds, xw + 4096u); w2[1] = lds16(lds, xw + 5120u);
    SCHED();
    T3(K0);
    acc[3] = smfma(ma[K0 + 1], cat16(wsh[2], wsh[3]), acc[3], sidx);
    SCHED();
    MA(3);
    if (!HI) { w3[0] = lds16(lds, xw + 6144u); w3[1] = lds16(lds, xw + 7168u); } else { MA(4); MA(5); }
    SCHED();
    T3(K0 + 1);
    acc[3] = smfma(ma[K0 + 2], cat16(w2[0], w2[1]), acc[3], sidx);
    SCHED();
    if (!HI) { MA(4); MA(5); MA(6); } else { MA(6); kq0 = lds16(lds, km0); kq1 = lds16(lds, km1); }
    SCHED();
    T3(K0 + 2);
    if (!HI) {
        acc[3] = smfma(ma[3], cat16(w3[0], w3[1]), acc[3], sidx);
        SCHED();
        kq0 = lds16(lds, km0); kq1 = lds16(lds, km1);
        SCHED();
        T3(3);
    }
    SCHED();
    kt1[0] = lds16(lds, kx1); kt1[1] = lds16(lds, kx1 + 1024u);
    SCHED();
#pragma unroll
    for (int i = 0; i < NB; ++i) acc[0] = smfma(ma[K1 + i], wr[0][K1 + i], acc[0], sidx);
    acc[1] = smfma(ma[K1], wr[1][K1], acc[1], sidx);
    acc[1] = smfma(ma[K1 + 1], wr[1][K1 + 1], acc[1], sidx);
    SCHED();
    {
        f32x4 y;
#pragma unroll
        for (int r = 0; r < 4; ++r) y[r] = half_add(acc[3][r], acc[0][r]);
        sts8(lds, ttw, pk4(y));
    }
    const bf16x8 tb0 = cat8(lds_tr(lds, ttr), lds_tr(lds, ttr + 128u));
    SCHED();
#pragma unroll
    for (int i = 2; i < NB; ++i) acc[1] = smfma(ma[K1 + i], wr[1][K1 + i], acc[1], sidx);
#pragma unroll
    for (int i = 0; i < NB; ++i) acc[2] = smfma(ma[K1 + i], wr[2][K1 + i], acc[2], sidx);
    SCHED();
    sa[0] = mfma(as8(kq0), tb0, ZERO4); sa[1] = mfma(as8(kt0[0]), tb0, ZERO4); sa[2] = mfma(as8(kt0[1]), tb0, ZERO4);
    SCHED();
    {
        f32x4 y;
#pragma unroll
        for (int r = 0; r < 4; ++r) y[r] = half_add(acc[1][r], acc[2][r]);
        sts8(lds, ttw + 512u, pk4(y));
    }
    const bf16x8 tb1 = cat8(lds_tr(lds, ttr + 512u), lds_tr(lds, ttr + 640u));
    sa[1] = mfma(as8(kt1[0]), tb1, sa[1]); sa[2] = mfma(as8(kt1[1]), tb1, sa[2]); sa[0] = mfma(as8(kq1), tb1, sa[0]);
#undef MA
#undef T3
}
template <int HI>
DI void role_ms_t(const Params& p, rsrc_t ws, int L, int h, unsigned char* lds) {
    constexpr unsigned MS_OFF = 0, MK_OFF = 7168, SP_OFF = 14336, SPW = 1408, EX_OFF = 26112, TT_OFF = 26624, XN_OFF = 34816, W_OFF = 63488;
    const int tid = threadIdx.x, w = __builtin_amdgcn_readfirstlane(tid >> 6), l = tid & 63, g = l >> 4, r16 = l & 15;
    const unsigned SB = OFF_ST + (unsigned)L * L_TOTAL;
    const u32x4* fa = (const u32x4*)(p.ws + OFF_FA + (size_t)h * 28 * 14 * 1024);
    bf16x16 wr[3][7];
#pragma unroll
    for (int t = 0; t < 3; ++t)
#pragma unroll
        for (int ks = 0; ks < 7; ++ks)
            wr[t][ks] = cat16(fa[((3 * w + t) * 14 + 2 * ks) * 64 + l], fa[((3 * w + t) * 14 + 2 * ks + 1) * 64 + l]);
#pragma unroll
    for (int k = 0; k < 7; ++k) sts16(lds, W_OFF + (tid + 512 * k) * 16u, fa[24 * 14 * 64 + tid + 512 * k]);
    __syncthreads();
    const int sidx = (r16 & 8) ? 0xEEEE : 0x4444;
    const unsigned ttw = TT_OFF + (unsigned)(w * 1024 + r16 * 32 + g * 8);
    const unsigned ttr = TT_OFF + (unsigned)(w * 1024 + (8 * (g & 1) + (r16 >> 2)) * 32 + ((r16 & 1) + 2 * (g >> 1)) * 8);
    const unsigned xw = W_OFF + ((unsigned)(w & 3) * 14u + (HI ? 8u : 0u)) * 1024u + l * 16u;
    const int n0 = (g >> 1) ? 3 * w : 24 + (w & 3), n1 = 3 * w + 1 + (g >> 1);
    const unsigned kx0 = XN_OFF + (unsigned)((n0 >> 1) * 2048 + ((n0 & 1) * 2 + (g & 1)) * 256 + r16 * 16);
    const unsigned kx1 = XN_OFF + (unsigned)((n1 >> 1) * 2048 + ((n1 & 1) * 2 + (g & 1)) * 256 + r16 * 16);
    const unsigned km0 = MK_OFF + (unsigned)((n0 * 2 + (g & 1)) * 128 + (r16 & 7) * 16);
    const unsigned km1 = MK_OFF + (unsigned)((n1 * 2 + (g & 1)) * 128 + (r16 & 7) * 16);
    const int mq = (tid >> 2) & 7, mQ = 7 * (tid >> 5) + mq;
    const unsigned mkw = MK_OFF + (unsigned)((mQ >> 1) * 128 + (tid & 3) * 32 + (mQ & 1) * 8);
    const unsigned spw = SP_OFF + (unsigned)w * SPW + (unsigned)(((r16 >> 2) & 1) * 704 + (r16 & 3) * 16);
    const unsigned spr = SP_OFF + (unsigned)((w & 1) * 704 + (l < 40 ? l : 32 + (l & 7)) * 16);
    const unsigned mu = (unsigned)tid * 16u;
    unsigned pre = PRE_MS_MEM, cadd = 0, s_sp = 0, s_r0 = 0, s_ta = 0, s_tb = 0;
    int dnom = 0, tadd = 0;
    bool s_wt = false;
    const unsigned xo = w >= 2 ? (unsigned)(tid - 128) * 16u : NOWHERE, xo4 = w >= 2 ? (unsigned)((tid - 128) & 255) * 16u : NOWHERE;
    if (w >= 2 && !fetch_xn6(ws, SB + L_XN, lds, XN_OFF, tid - 128, w)) return;
    unsigned t0 = now() - PRE_MS_MEM;
    for (int s = 0; s < NSEG - 1; ++s) {
        TR(0);
        STAT(0, st_a); STAT(1, st_b); STAT(2, st_c); STAT(3, st_d);
        __syncthreads();
        TR(1);
        u32x4 wsh[4], kt0[2];
#pragma unroll
        for (int k = 0; k < 4; ++k) wsh[k] = lds16(lds, xw + (unsigned)k * 1024u);
        kt0[0] = lds16(lds, kx0); kt0[1] = lds16(lds, kx0 + 1024u);
        {
            const unsigned src = SB + L_MEM + (unsigned)s * MEM_SZ + (MREP == 2 && s > 0 && h >= 8 ? 8192u : 0u);
            bool waited = pace(t0, pre + cadd);
            if (s == 1) {
                unsigned have = 0;
                if (!wait_flag(ws, (unsigned)L * FLG_L + FLG_M, 2u, have)) return;
                __builtin_amdgcn_s_sleep(HINT_GAP);
                waited = false;
            }
            const unsigned r0 = stamp();
            u32x4 m = ld16c(ws, mu, src);
            unsigned spins = 0;
            mem_stage<true, false>(m, lds, MS_OFF, 0, tid);
            if (mq != 7) { sts8(lds, mkw, (u32x2){m.x, m.y}); sts8(lds, mkw + 16u, (u32x2){m.z, m.w}); }
            if (__builtin_expect(!none_sent(mx4(m)), 0)) {
                while (!none_sent(mx4(m))) {
                    if (!spin_ok(spins, ws)) return;
                    m = ld16(ws, mu, src);
                    __builtin_amdgcn_s_waitcnt(0x0F70);
                }
                mem_stage<true, false>(m, lds, MS_OFF, 0, tid);
                if (mq != 7) { sts8(lds, mkw, (u32x2){m.x, m.y}); sts8(lds, mkw + 16u, (u32x2){m.z, m.w}); }
            }
            const unsigned ta = __builtin_amdgcn_readlane(m.x, 28), tb = __builtin_amdgcn_readlane(m.x, 60);
            sts4(lds, EX_OFF + (unsigned)w * 4u, later(ta, tb) | ((ta | tb) & MARK));
            TR(2);
            __syncthreads();
            s_sp = spins; s_r0 = r0; s_ta = ta; s_tb = tb; s_wt = waited;
        }
        TR(3);
        f32x4 sa[3];
        ms_scores<HI>(lds, wr, wsh, kt0, xw, sidx, MS_OFF + l * 16u, ttw, ttr, kx1, km0, km1, sa);
        if (r16 < 8) {
            stsf4(lds, spw + (unsigned)g * 128u, sa[1]);
            stsf4(lds, spw + (unsigned)g * 128u + 64u, sa[2]);
            if (g < 2) stsf4(lds, spw + 512u + (unsigned)g * 64u, sa[0]);
        }
        TR(4);
        __syncthreads();
        TR(5);
        t0 = now();
        if (!HI && w < 2) {
            const unsigned ts = stamp();
            f32x4 q[8];
#pragma unroll
            for (int ww = 0; ww < 8; ++ww) q[ww] = ldsf4(lds, spr + (unsigned)ww * SPW);
            const f32x4 a = ((q[0] + q[1]) + (q[2] + q[3])) + ((q[4] + q[5]) + (q[6] + q[7]));
            float mx = fmaxf(fmaxf(a[0], a[1]), fmaxf(a[2], a[3]));
            mx = fmaxf(mx, dpp<0x124>(mx)); mx = fmaxf(mx, dpp<0x128>(mx));
            mx = xg_max(mx) * LOG2E;
            f32x4 e;
#pragma unroll
            for (int r = 0; r < 4; ++r) e[r] = __builtin_amdgcn_exp2f(__builtin_fmaf(a[r], LOG2E, -mx));
            float part = l < 40 ? (e[0] + e[1]) + (e[2] + e[3]) : 0.f;
            part += dpp<0x124>(part); part += dpp<0x128>(part);
            const float inv = __builtin_amdgcn_rcpf(xg_sum(part));
            u32x2 pa = pk4s(e * inv);
            unsigned nx = (unsigned)__builtin_amdgcn_update_dpp(0, (int)pa.x, 0x104, 0xf, 0xf, true);
            unsigned ny = (unsigned)__builtin_amdgcn_update_dpp(0, (int)pa.y, 0x104, 0xf, 0xf, true);
            if (l >= 40) pa.x = ts;
            if (l < 48 && (PREP || !(l & 4)))
                st16c(ws, (unsigned)((l >> 3) * 64 + (l & 3) * 16 + (l & 4) * 3072), SB + L_PM + (unsigned)s * PM_SZ + (unsigned)h * 768u + (unsigned)w * 384u,
                     (u32x4){pa.x, pa.y, nx, ny});
            TR(6);
        }
        asm volatile("" ::: "memory");
        if (s == NSEG - 2) break;
        if (w >= 2) __builtin_amdgcn_s_sleep(XN_DELAY);
        const unsigned xsrc = SB + L_XN + (unsigned)(ISO(s + 1)) * XN_SZ;
        u32x4 xn[5];
#pragma unroll
        for (int k = 0; k < 4; ++k) xn[k] = ld16(ws, xo, xsrc + k * 6144u);
        xn[4] = ld16(ws, xo4, xsrc + 24576u);
        SCHED();
        {
            const unsigned e = wg_latest(lds, EX_OFF);
            const int dur = (int)((stamp() - e) << 2) >> 2;
            const bool marked = (e & MARK) != 0u;
            if (TADD && !marked && s_sp != 0u && s > 8) tadd = min(tadd + TADD, 4 * TADD);
            if (!marked && (cadd == 0u || s_sp != 0u)) pre = steer(pre, s_wt, s_sp, s_r0, WGM && s_ta ? e : s_ta, WGM && s_tb ? e : s_tb, TGT_MEM + tadd);
            if (!marked && s_sp == 0u && s > 1) dnom = dnom == 0 ? dur * 4 : dnom + dur - (dnom >> 2);
            cadd = marked ? (unsigned)min(max(PEN - (dnom == 0 ? 0 : dur - (dnom >> 2)) * 23, 0), PEN) : 0u;
        }
        SCHED();
        if (w >= 2) {
            if (!none_sent(max(max(max(mx4(xn[0]), mx4(xn[1])), max(mx4(xn[2]), mx4(xn[3]))), mx4(xn[4])))) {
                unsigned spins = 1;
                if (!probe(ws, xsrc + (unsigned)w * 1024u, spins) || !fetch_xn6(ws, xsrc, lds, XN_OFF, tid - 128, w)) return;
            } else
            {
#pragma unroll
                for (int k = 0; k < 4; ++k) sts16(lds, XN_OFF + xo + k * 6144u, xn[k]);
                if (w < 6) sts16(lds, XN_OFF + 24576u + xo4, xn[4]);
            }
        }
    }
}
DI void role_ms(const Params& p, rsrc_t ws, int L, int h, unsigned char* lds) {
    if (__builtin_amdgcn_readfirstlane(threadIdx.x >> 8) == 0) role_ms_t<0>(p, ws, L, h, lds); else role_ms_t<1>(p, ws, L, h, lds);
}

DI void role_v(const Params& p, rsrc_t ws, int L, int v, unsigned char* lds) {
    constexpr unsigned MS_OFF = 0, Y_OFF = 7168, OP_OFF = 15360, W2_OFF = 23552, W3_OFF = W2_OFF + 65536, EX_OFF = W3_OFF + 57344, FL_OFF = EX_OFF + 32, EY_OFF = FL_OFF + 32;
    const int tid = threadIdx.x, w = __builtin_amdgcn_readfirstlane(tid >> 6), l = tid & 63, g = l >> 4, r16 = l & 15;
    const unsigned SB = OFF_ST + (unsigned)L * L_TOTAL;
    const u32x4* fb = (const u32x4*)(p.ws + OFF_FB);
    auto wunit = [&](int tile, int kk, int gg, int n16) -> u32x4 {
        const int wc = tile * 16 + n16, hh = wc / 28, c = 28 * v + wc % 28;
        return fb[(((c >> 4) * 16 + hh) * 14 + kk) * 64 + gg * 16 + (c & 15)];
    };
#pragma unroll
    for (int k = 0; k < 8; ++k) sts16(lds, W2_OFF + (unsigned)((w * 8 + k) * 1024 + l * 16), wunit(16 + w, 6 + k, g, r16));
#pragma unroll
    for (int k = 0; k < 7; ++k) {
        const int u = tid + 512 * k;
        sts16(lds, W3_OFF + u * 16u, wunit(24 + u / 896, (u / 64) % 14, (u >> 4) & 3, u & 15));
    }
    if (tid == 0) sts4(lds, FL_OFF, 0u);
    bf16x16 wr[2][7], wr2[3];
#pragma unroll
    for (int t = 0; t < 2; ++t)
#pragma unroll
        for (int ks = 0; ks < 7; ++ks) wr[t][ks] = cat16(wunit(2 * w + t, 2 * ks, g, r16), wunit(2 * w + t, 2 * ks + 1, g, r16));
#pragma unroll
    for (int ks = 0; ks < 3; ++ks) wr2[ks] = cat16(wunit(16 + w, 2 * ks, g, r16), wunit(16 + w, 2 * ks + 1, g, r16));
    const int sidx = (r16 & 8) ? 0xEEEE : 0x4444;
    const int ha = 2 * w;
    unsigned uoff[2]; unsigned yoff[2];
#pragma unroll
    for (int t = 0; t < 2; ++t) {
        int cl = 16 * t + r16; if (cl > 27) cl = 27;
        const int c = 28 * v + cl;
        uoff[t] = (unsigned)(((c >> 4) * 16 * 64 + g * 16 + (c & 15)) * 16 + ha * 1024);
        yoff[t] = Y_OFF + (unsigned)(((ha + (g >> 1)) * 28 + cl) * 16 + (g & 1) * 8);
    }
    const unsigned ptok = (unsigned)(ha * 768 + ((r16 >> 2) & 1) * 384 + g * 64 + (r16 & 3) * 16);
    const unsigned pmem = (unsigned)((ha + (g >> 1)) * 768 + ((r16 >> 2) & 1) * 384 + 256 + (r16 >> 3) * 64 + (r16 & 3) * 16 + (g & 1) * 8);
    const unsigned ys0 = Y_OFF + (unsigned)(((2 * w + (g >> 1)) * 16 + r16) * 16 + (g & 1) * 8);
    const unsigned ys1 = Y_OFF + (unsigned)((((g >> 1) ? 24 + w : 16 + w) * 16 + r16) * 16 + (g & 1) * 8);
    const unsigned w2 = W2_OFF + (unsigned)(w * 8192 + l * 16), w3 = W3_OFF + (unsigned)((w & 3) * 14336 + l * 16);
    const int ct0 = (28 * v) >> 4, ct1 = (28 * v + 27) >> 4;
    const unsigned fU = (unsigned)L * FLG_L + FLG_U + (unsigned)((min(ct0 + min(l, 2), ct1) * 8 + w) * 128);
    unsigned haveU = 0;
    const unsigned mu = (unsigned)tid * 16u;
    const unsigned mso = (w == 0 && !(l & 1)) ? (unsigned)l * 8u : (MREP && w == 0) ? (unsigned)l * 8u + 8184u : NOWHERE, hso = (v == 0 && tid == 0) ? 0u : NOWHERE;
    unsigned prm = PRE_V_MEM, pre = PRE_V_PM, m_sp = 0, m_r0 = 0, m_ta = 0, m_tb = 0;
    bool m_wt = false;
    __syncthreads();
    unsigned t0 = now() - PRE_V_PM;

    for (int s = 0; s < NSEG; ++s) {
        TR(0);
        const int su = ISO(s < NSEG - 1 ? s : NSEG - 2);
        const unsigned ub = SB + L_U + (unsigned)su * U_SZ;
        const unsigned fu = ld4(__builtin_amdgcn_make_buffer_rsrc(p.ws, 0, haveU < (unsigned)su + 1u ? WS_NEED : 0u, 0x00020000), fU, OFF_FLG);
        u32x4 w2p[8], w3p[W3P];
        {
            const unsigned src = SB + L_MEM + (unsigned)s * MEM_SZ + (s > 0 && (MREP == 1 || (MREP == 2 && v >= 8)) ? 8192u : 0u);
            const bool waited = pace(t0, prm);
            const unsigned r0 = stamp();
            TR(1); TW(0);
            u32x4 m = ld16(ws, mu, src);
            SCHED();
#pragma unroll
        for (int k = 0; k < 8; ++k) w2p[k] = lds16(lds, w2 + k * 1024u);
#pragma unroll
        for (int k = 0; k < W3P; ++k) w3p[k] = lds16(lds, w3 + (6 + k) * 1024u);
            SCHED();
            unsigned spins = 0;
            while (!none_sent(mx4(m))) {
                __builtin_amdgcn_s_sleep(LAZY_GAP);
                if (!spin_ok(spins, ws)) return;
                m = ld16(ws, mu, src);
                __builtin_amdgcn_s_waitcnt(0x0F70);
            }
            mem_stage<true, false>(m, lds, MS_OFF, 0, tid);
            TW(1);
            haveU = max(haveU, min((unsigned)__builtin_amdgcn_readlane(fu, 0), min((unsigned)__builtin_amdgcn_readlane(fu, 1), (unsigned)__builtin_amdgcn_readlane(fu, 2))));
            const unsigned ta = __builtin_amdgcn_readlane(m.x, 28), tb = __builtin_amdgcn_readlane(m.x, 60);
            sts4(lds, EX_OFF + (unsigned)w * 4u, (ta | tb) & MARK);
            m_sp = spins; m_r0 = r0; m_ta = ta; m_tb = tb; m_wt = waited;
        }
        asm volatile("" ::: "memory");
        u32x4 uf[2][2];
        bool early = haveU >= (unsigned)su + 1u;
        if (early) {
#pragma unroll
            for (int t = 0; t < 2; ++t) { uf[0][t] = ld16(ws, uoff[t], ub); uf[1][t] = ld16(ws, uoff[t], ub + 1024u); }
        }
        TR(2); TW(2);
        __syncthreads();
        TW(3);
        STAT(0, st_a); STAT(1, st_b); STAT(2, st_c); STAT(3, st_d); STAT(4, st_e); STAT(5, st_f); STAT(6, st_g); STAT(7, st_h);
        if (s == 0 && v == 0 && w == 7) post_flag(ws, (unsigned)L * FLG_L + FLG_M, (unsigned)s + 1u, l);
        asm volatile("" ::: "memory");
        const unsigned fu2 = ld4(__builtin_amdgcn_make_buffer_rsrc(p.ws, 0, early ? 0u : WS_NEED, 0x00020000), fU, OFF_FLG);
        f32x4 acc[4];
#pragma unroll
        for (int t = 0; t < 4; ++t) acc[t] = ZERO4;
#define YLOOP(FOUR) _Pragma("unroll") for (int i = 0; i < 7; ++i) { \
            const int ks = (i + 3) % 7; \
            const bf16x8 ma = as8(lds16(lds, MS_OFF + ks * 1024u + l * 16u)); \
            if (FOUR) acc[3] = smfma(ma, 2 * i + 1 < W3P ? cat16(w3p[(2 * i) % W3P], w3p[(2 * i + 1) % W3P]) \
                                         : cat16(lds16(lds, w3 + ks * 2048u), lds16(lds, w3 + ks * 2048u + 1024u)), acc[3], sidx); \
            acc[2] = smfma(ma, ks < 3 ? wr2[ks % 3] : cat16(w2p[(2 * i) & 7], w2p[(2 * i + 1) & 7]), acc[2], sidx); \
            acc[0] = smfma(ma, wr[0][ks], acc[0], sidx); acc[1] = smfma(ma, wr[1][ks], acc[1], sidx); }
        if (w < 4) { YLOOP(true) } else { YLOOP(false) }
        if (!early) {
            asm volatile("" ::: "memory");
            haveU = max(haveU, min((unsigned)__builtin_amdgcn_readlane(fu2, 0), min((unsigned)__builtin_amdgcn_readlane(fu2, 1), (unsigned)__builtin_amdgcn_readlane(fu2, 2))));
            early = haveU >= (unsigned)su + 1u && s > 0;
            if (early) {
#pragma unroll
                for (int t = 0; t < 2; ++t) { uf[0][t] = ld16(ws, uoff[t], ub); uf[1][t] = ld16(ws, uoff[t], ub + 1024u); }
            }
        }
        {
            f32x4 y0, y1;
#pragma unroll
            for (int r = 0; r < 4; ++r) { y0[r] = half_add(acc[0][r], acc[1][r]); y1[r] = half_add(acc[2][r], acc[3][r]); }
            sts8(lds, ys0, pk4(y0)); sts8(lds, ys1, pk4(y1));
        }
        TW(4);
        __syncthreads();
        TW(5);
        const u32x2 yf0 = lds8(lds, yoff[0]), yf1 = lds8(lds, yoff[1]);
        const unsigned mk = *(const unsigned*)(lds + EX_OFF + (l & 7) * 4);
        const unsigned yb_ = SB + L_Y + (unsigned)s * Y_SZ + (unsigned)v * 8192u;
        st16(ws, mu, yb_, (u32x4){nosent(yf0.x), nosent(yf0.y), nosent(yf1.x), nosent(yf1.y)});
        asm volatile("" ::: "memory");
        if (s == NSEG - 1) break;
        if (!early) {
            if (!wait_flag(ws, fU, (unsigned)su + 1u, haveU)) return;
#pragma unroll
            for (int t = 0; t < 2; ++t) { uf[0][t] = ld16(ws, uoff[t], ub); uf[1][t] = ld16(ws, uoff[t], ub + 1024u); }
            if (s > 0 && now() - t0 > pre) sts4(lds, FL_OFF, (unsigned)s + 1u);
            if (s == 0) sleep_until(now(), L == 0 ? GATE0 : GATE1);
        }
        u32x4 ya = {yf0.x, yf0.y, 0u, 0u}, yb = {yf1.x, yf1.y, 0u, 0u};
        TR(3); TW(6);
        const unsigned pbase = SB + L_PM + (unsigned)s * PM_SZ + (PREP && v >= 8 ? 12288u : 0u);
        const unsigned cadd = __builtin_amdgcn_ballot_w64(mk != 0u) != 0ull ? PEN : 0u;
        const bool waited = pace(t0, pre + cadd);
        const unsigned r0 = stamp();
        TW(7);
        u32x4 pa = ld16c(ws, ptok, pbase), pb = ld16c(ws, ptok, pbase + 768u);
        u32x2 qm = ld8c(ws, pmem, pbase);
        SCHED();
        if (!((m_ta | m_tb) & MARK)) prm = steer(prm, m_wt, m_sp, m_r0, m_ta, m_tb, TGT_MEM + VEXTRA);
        SCHED();
        unsigned spins = 0;
        if (__builtin_expect(!none_sent(max(max(mx4(pa), mx4(pb)), max(qm.x, qm.y))), 0)) {
            while (!none_sent(max(max(mx4(pa), mx4(pb)), max(qm.x, qm.y)))) {
                if (!spin_ok(spins, ws)) return;
                pa = ld16(ws, ptok, pbase); pb = ld16(ws, ptok, pbase + 768u); qm = ld8(ws, pmem, pbase);
                __builtin_amdgcn_s_waitcnt(0x0F70);
            }
            sts4(lds, FL_OFF, (unsigned)s + 1u);
        }
        TR(4); TW(8);
        {
            const u32x4 q4 = {qm.x, qm.y, 0u, 0u};
            f32x4 o0 = mfma(as8(uf[0][0]), as8(pa), ZERO4), o1 = mfma(as8(uf[0][1]), as8(pa), ZERO4);
            o0 = mfma(as8(uf[1][0]), as8(pb), o0); o1 = mfma(as8(uf[1][1]), as8(pb), o1);
            o0 = mfma(as8(ya), as8(q4), o0); o1 = mfma(as8(yb), as8(q4), o1);
            if (r16 < 8) {
                stsf4(lds, OP_OFF + (unsigned)((w * 64 + g * 8 + r16) * 16), o0);
                stsf4(lds, OP_OFF + (unsigned)((w * 64 + 32 + g * 8 + r16) * 16), o1);
            }
        }
        __syncthreads();
        TR(5);
        t0 = now();
        u32x4 md = {0u, 0u, 0u, 0u};
        if (w == 0) {
            const unsigned ts = stamp();
            const unsigned fl = *(const unsigned*)(lds + FL_OFF);
            const int lc = l < 56 ? l : 55;
            f32x4 o[8];
#pragma unroll
            for (int ww = 0; ww < 8; ++ww) o[ww] = ldsf4(lds, OP_OFF + (unsigned)((ww * 64 + lc) * 16));
            const f32x4 r = ((o[0] + o[1]) + (o[2] + o[3])) + ((o[4] + o[5]) + (o[6] + o[7]));
            u32x2 pr = pk4s(r);
            const unsigned nx = (unsigned)__builtin_amdgcn_update_dpp(0, (int)pr.x, 0xF5, 0xf, 0xf, true);
            const unsigned ny = (unsigned)__builtin_amdgcn_update_dpp(0, (int)pr.y, 0xF5, 0xf, 0xf, true);
            if (l >= 56) pr.x = fl == (unsigned)s + 1u ? ts | MARK : ts;
            md = (u32x4){pr.x, pr.y, nx, ny};
        }
        st16c(ws, mso, SB + L_MEM + (unsigned)(s + 1) * MEM_SZ + (unsigned)v * 512u, md);
        TR(6); TW(9);
        st4(ws, hso, OFF_FLG + (unsigned)L * FLG_L + FLG_M, (unsigned)s + 2u);
        {
            u32x4 z = SENT4;
            asm volatile("" : "+v"(z));
            st16(ws, s + ARM_K < NSEG ? mu : NOWHERE, yb_ + ARM_K * Y_SZ, z);
        }
        if (cadd == 0u || spins != 0u) pre = steer(pre, waited, spins, r0, later(__builtin_amdgcn_readlane(qm.x, 8), __builtin_amdgcn_readlane(qm.x, 12)),
                    later(__builtin_amdgcn_readlane(qm.x, 40), __builtin_amdgcn_readlane(qm.x, 44)), TGT_PM);
    }
}

DI void role_ts(const Params& p, rsrc_t ws, int L, int y, unsigned char* lds) {
    constexpr unsigned XN_OFF = 0, MK_OFF = 28672, SP_OFF = 35840, W_OFF = 76800;
    const int h = y >> 1;
    const int tid = threadIdx.x, w = __builtin_amdgcn_readfirstlane(tid >> 6), l = tid & 63, g = l >> 4, r16 = l & 15;
    const unsigned SB = OFF_ST + (unsigned)L * L_TOTAL;
    const u32x4* fa = (const u32x4*)(p.ws + OFF_FA + (size_t)h * 28 * 14 * 1024);
    bf16x8 wr[3][14];
#pragma unroll
    for (int t = 0; t < 3; ++t)
#pragma unroll
        for (int kk = 0; kk < 14; ++kk) wr[t][kk] = as8(fa[((3 * w + t) * 14 + kk) * 64 + l]);
#pragma unroll
    for (int k = 0; k < 7; ++k) sts16(lds, W_OFF + (tid + 512 * k) * 16u, fa[24 * 14 * 64 + tid + 512 * k]);
    const int nt3 = 24 + (w & 3);
    const unsigned xw = W_OFF + (unsigned)(w & 3) * 14336u + l * 16u, xx = XN_OFF + (unsigned)(w >> 2) * 1024u + l * 16u;

    unsigned haveM = 0;
    for (int s = y & 1; s < NSEG; s += 2) {
        TR(0);
        unsigned late;
        if (!fetch_xn(ws, SB + L_XN + (unsigned)s * XN_SZ, lds, XN_OFF, tid, w, late)) return;
        __syncthreads();
        TR(1);
        u32x2 tf[2][4];
#pragma unroll
        for (int mt = 0; mt < 2; ++mt) {
            f32x4 acc[3];
#pragma unroll
            for (int t = 0; t < 3; ++t) acc[t] = ZERO4;
#pragma unroll
            for (int kk = 0; kk < 14; ++kk) {
                const bf16x8 xb = as8(lds16(lds, XN_OFF + (unsigned)((kk * 2 + mt) * 1024 + l * 16)));
#pragma unroll
                for (int t = 0; t < 3; ++t) acc[t] = mfma(wr[t][kk], xb, acc[t]);
            }
#pragma unroll
            for (int t = 0; t < 3; ++t) tf[mt][t] = pk4(acc[t]);
        }
        {
            f32x4 acc = ZERO4;
#pragma unroll
            for (int kk = 0; kk < 14; ++kk)
                acc = mfma(as8(lds16(lds, xw + (unsigned)kk * 1024u)), as8(lds16(lds, xx + (unsigned)kk * 2048u)), acc);
            const u32x2 t3 = pk4(acc), z = {0u, 0u};
            tf[0][3] = w < 4 ? t3 : z; tf[1][3] = w < 4 ? z : t3;
        }
        f32x4 sa[5];
#pragma unroll
        for (int k = 0; k < 5; ++k) sa[k] = ZERO4;
#pragma unroll
        for (int pr = 0; pr < 2; ++pr) {
            const int na = 3 * w + 2 * pr, nb = pr == 0 ? 3 * w + 1 : nt3;
            const bf16x8 k0 = cat8(xn_key(lds, XN_OFF, na, 0, g, r16), xn_key(lds, XN_OFF, nb, 0, g, r16));
            const bf16x8 k1 = cat8(xn_key(lds, XN_OFF, na, 1, g, r16), xn_key(lds, XN_OFF, nb, 1, g, r16));
            const bf16x8 t0 = cat8(tf[0][2 * pr], tf[0][2 * pr + 1]), t1 = cat8(tf[1][2 * pr], tf[1][2 * pr + 1]);
            sa[2] = mfma(k0, t0, sa[2]); sa[3] = mfma(k0, t1, sa[3]); sa[4] = mfma(k1, t1, sa[4]);
        }
#pragma unroll
        for (int k = 2; k < 5; ++k) stsf4(lds, SP_OFF + (unsigned)(((w * 5 + k) * 64 + l) * 16), sa[k]);
        TR(2);
        if (!wait_flag(ws, (unsigned)L * FLG_L + FLG_M, (unsigned)s + 1u, haveM)) return;
        __builtin_amdgcn_s_sleep(TSHINT);
        if (!fetch_mem<false, true, true>(ws, SB + L_MEM + (unsigned)s * MEM_SZ + (MREP && s > 0 ? 8192u : 0u), lds, 0, MK_OFF, tid, w)) return;
        __syncthreads();
        TR(3);
#pragma unroll
        for (int pr = 0; pr < 2; ++pr) {
            const int na = 3 * w + 2 * pr, nb = pr == 0 ? 3 * w + 1 : nt3;
            const bf16x8 km = cat8(mem_key(lds, MK_OFF, na, g, r16), mem_key(lds, MK_OFF, nb, g, r16));
            sa[0] = mfma(km, cat8(tf[0][2 * pr], tf[0][2 * pr + 1]), sa[0]);
            sa[1] = mfma(km, cat8(tf[1][2 * pr], tf[1][2 * pr + 1]), sa[1]);
        }
#pragma unroll
        for (int k = 0; k < 2; ++k) stsf4(lds, SP_OFF + (unsigned)(((w * 5 + k) * 64 + l) * 16), sa[k]);
        __syncthreads();
        {
            const int row = tid >> 4, q = tid & 15, mt = row >> 4, i = row & 15;
            const int qq = q < 10 ? q : 9;
            const int kt = qq < 2 ? 0 : (qq < 6 ? 1 : 2), gq = qq < 2 ? qq : ((qq - 2) & 3);
            const bool live = (q < 10) && !(kt == 2 && mt == 0);
            const int tile = kt == 0 ? mt : (kt == 1 ? 2 + mt : 4);
            f32x4 sv = ZERO4;
#pragma unroll
            for (int ww = 0; ww < 8; ++ww) sv += ldsf4(lds, SP_OFF + (unsigned)(((ww * 5 + tile) * 64 + gq * 16 + i) * 16));
            bool lv[4];
#pragma unroll
            for (int r = 0; r < 4; ++r) {
                const int key = (kt - 1) * 16 + 4 * gq + r;
                lv[r] = live && (kt == 0 || key <= row);
                sv[r] = lv[r] ? sv[r] : -3.0e38f;
            }
            const float mx = row16_max(fmaxf(fmaxf(sv[0], sv[1]), fmaxf(sv[2], sv[3])));
            f32x4 e;
#pragma unroll
            for (int r = 0; r < 4; ++r) e[r] = lv[r] ? __builtin_amdgcn_exp2f((sv[r] - mx) * LOG2E) : 0.f;
            const float inv = __builtin_amdgcn_rcpf(row16_sum((e[0] + e[1]) + (e[2] + e[3])));
            e *= inv;
            const unsigned po = (unsigned)h * 2560u + (unsigned)mt * 1280u +
                                (kt == 0 ? 1024u + (unsigned)(gq * 16 + i) * 8u : (unsigned)(gq * 16 + i) * 16u + (unsigned)(kt - 1) * 8u);
            if (q < 10) st8(ws, po, SB + L_PT + (unsigned)s * PT_SZ, pk4s(e));
            if (q < 10 && s + ARM_K < NSEG) st8(ws, po, SB + L_PT + ((unsigned)s + ARM_K) * PT_SZ, (u32x2){SENT, SENT});
            TR(6);
        }
    }
}

DI void role_vu(const Params& p, rsrc_t ws, int L, int ct, unsigned char* lds) {
    const int tid = threadIdx.x, w = __builtin_amdgcn_readfirstlane(tid >> 6), l = tid & 63;
    const unsigned SB = OFF_ST + (unsigned)L * L_TOTAL;
    const u32x4* fb = (const u32x4*)(p.ws + OFF_FB + (size_t)ct * 16 * 14 * 1024);
    bf16x8 wr[2][14];
#pragma unroll
    for (int t = 0; t < 2; ++t)
#pragma unroll
        for (int kk = 0; kk < 14; ++kk) wr[t][kk] = as8(fb[((2 * w + t) * 14 + kk) * 64 + l]);
    const unsigned fU = (unsigned)L * FLG_L + FLG_U + (unsigned)((ct * 8 + w) * 128);
    const unsigned uo = (unsigned)((ct * 16 + 2 * w) * 1024 + l * 16);
    unsigned pf = SENT;
    for (int s = 0; s < NSEG; ++s) {
        TR(0);
        const unsigned xo = (unsigned)(s & 1) * XN_SZ, src = SB + L_XN + (unsigned)s * XN_SZ;
        {
            unsigned spins = 1;
            if (uni(pf) == SENT && !probe(ws, src + (unsigned)w * 1024u, spins)) return;
            XnRegs x;
            for (;;) {
                xn_issue(ws, src, tid, x);
                if (xn_ok(x)) break;
                if (!spin_ok(spins, ws)) return;
            }
            xn_stage(x, lds, xo, tid);
        }
        __syncthreads();
        TR(1);
        f32x4 acc[2][2];
#pragma unroll
        for (int t = 0; t < 2; ++t) { acc[t][0] = ZERO4; acc[t][1] = ZERO4; }
#pragma unroll
        for (int kk = 0; kk < 14; ++kk)
#pragma unroll
            for (int mt = 0; mt < 2; ++mt) {
                const bf16x8 xa = as8(lds16(lds, xo + (unsigned)((kk * 2 + mt) * 1024 + l * 16)));
#pragma unroll
                for (int t = 0; t < 2; ++t) acc[t][mt] = mfma(xa, wr[t][kk], acc[t][mt]);
            }
#pragma unroll
        for (int t = 0; t < 2; ++t) {
            const u32x2 a = pk4(acc[t][0]), b = pk4(acc[t][1]);
            st16(ws, uo, SB + L_U + (unsigned)s * U_SZ + t * 1024u, (u32x4){a.x, a.y, b.x, b.y});
        }
        pf = ld4(ws, 0, (s + 1 < NSEG ? src + XN_SZ : src) + (unsigned)w * 1024u);
        __builtin_amdgcn_s_waitcnt(0x0F70);
        asm volatile("" ::: "memory");
        post_flag(ws, fU, (unsigned)s + 1u, l);
        TR(6);
    }
}

DI void role_vo(const Params& p, rsrc_t ws, int L, int v, unsigned char* lds) {
    const int tid = threadIdx.x, w = __builtin_amdgcn_readfirstlane(tid >> 6), l = tid & 63, g = l >> 4, r16 = l & 15;
    const unsigned SB = OFF_ST + (unsigned)L * L_TOTAL;
    const int ha = 2 * w;
    unsigned uoff[2];
#pragma unroll
    for (int t = 0; t < 2; ++t) {
        int cl = 16 * t + r16; if (cl > 27) cl = 27;
        const int c = 28 * v + cl;
        uoff[t] = (unsigned)(((c >> 4) * 16 * 64 + g * 16 + (c & 15)) * 16 + ha * 1024);
    }
    const unsigned ptok = (unsigned)(ha * 2560 + l * 16), pmem = (unsigned)((ha + (g >> 1)) * 2560 + 1024 + ((g & 1) * 16 + r16) * 8);
    const int ct0 = (28 * v) >> 4, ct1 = (28 * v + 27) >> 4;
    const unsigned fU = (unsigned)L * FLG_L + FLG_U + (unsigned)((min(ct0 + min(l, 2), ct1) * 8 + w) * 128);
    unsigned haveU = 0;
    const unsigned pprobe = (unsigned)((ha + (l >> 1 & 1)) * 2560 + 1280 + 1024 + 248);
    const int row = (tid >> 3) & 31, qd = tid & 7, qc = qd < 7 ? qd : 6;
    const int Q = 7 * v + qc;
    const unsigned xmo = (unsigned)((Q >> 2) * 1024 + row * 32 + (Q & 3) * 8);
    const unsigned rdo = (unsigned)((((qc >> 2) * 2 + (row >> 4)) * 64 + (qc & 3) * 16 + (row & 15)) * 16);
    for (int s = 0; s < NSEG; ++s) {
        TR(0);
        if (!wait_flag(ws, fU, (unsigned)s + 1u, haveU)) return;
        u32x4 uf[2][2];
        {
            const unsigned ub = SB + L_U + (unsigned)s * U_SZ;
#pragma unroll
            for (int t = 0; t < 2; ++t) { uf[0][t] = ld16(ws, uoff[t], ub); uf[1][t] = ld16(ws, uoff[t], ub + 1024u); }
        }
        const unsigned pbase = SB + L_PT + (unsigned)s * PT_SZ;
        for (unsigned spins = 0;;) {
            const unsigned f = ld4(ws, pprobe, pbase);
            if ((unsigned)__builtin_amdgcn_readlane(f, 1) != SENT && (unsigned)__builtin_amdgcn_readlane(f, 2) != SENT) break;
            __builtin_amdgcn_s_sleep(VO_GAP);
            if (!spin_ok(spins, ws)) return;
        }
        asm volatile("" ::: "memory");
        TR(1);
        u32x4 pa[2], pb[2], yf; u32x2 qm[2], xr;
        {
            for (unsigned spins = 1;;) {
                yf = ld16(ws, (unsigned)tid * 16u, SB + L_Y + (unsigned)s * Y_SZ + (unsigned)v * 8192u);
#pragma unroll
                for (int mt = 0; mt < 2; ++mt) {
                    pa[mt] = ld16(ws, ptok, pbase + mt * 1280u); pb[mt] = ld16(ws, ptok, pbase + 2560u + mt * 1280u);
                    qm[mt] = ld8(ws, pmem, pbase + mt * 1280u);
                }
                xr = ld8(ws, xmo, SB + L_XMID + (unsigned)s * XN_SZ);
                bool ok = ok2(xr) & ok4(yf);
#pragma unroll
                for (int mt = 0; mt < 2; ++mt) ok = ok & ok4(pa[mt]) & ok4(pb[mt]) & ok2(qm[mt]);
                if (__all(ok)) break;
                if (!retry(ws, pbase + (unsigned)ha * 2560u, spins)) return;
            }
        }
        TR(2);
        const unsigned ob = (unsigned)(s & 1) * 32768u;
#pragma unroll
        for (int t = 0; t < 2; ++t)
#pragma unroll
            for (int mt = 0; mt < 2; ++mt) {
                f32x4 o = ZERO4;
                o = mfma(as8(uf[0][t]), as8(pa[mt]), o);
                o = mfma(as8(uf[1][t]), as8(pb[mt]), o);
                o = mfma16(t ? (u32x2){yf.z, yf.w} : (u32x2){yf.x, yf.y}, qm[mt], o);
                stsf4(lds, ob + (unsigned)((((w * 2 + t) * 2 + mt) * 64 + l) * 16), o);
            }
        __syncthreads();
        if (w < 4) {
            f32x4 o = ZERO4;
#pragma unroll
            for (int ww = 0; ww < 8; ++ww) o += ldsf4(lds, ob + rdo + (unsigned)ww * 4096u);
            f32x4 r;
            r[0] = bflo(xr.x) + bfr(o[0]); r[1] = bfhi(xr.x) + bfr(o[1]); r[2] = bflo(xr.y) + bfr(o[2]); r[3] = bfhi(xr.y) + bfr(o[3]);
            if (L == 0) {
                u32x2 pv = pk4s(r);
                const float a0 = bflo(pv.x), a1 = bfhi(pv.x), a2 = bflo(pv.y), a3 = bfhi(pv.y);
                float s1 = qd < 7 ? (a0 + a1) + (a2 + a3) : 0.f;
                float s2 = qd < 7 ? __builtin_fmaf(a0, a0, a1 * a1) + __builtin_fmaf(a2, a2, a3 * a3) : 0.f;
                s1 += dpp<0xB1>(s1); s2 += dpp<0xB1>(s2);
                s1 += dpp<0x4E>(s1); s2 += dpp<0x4E>(s2);
                s1 += dpp<0x141>(s1); s2 += dpp<0x141>(s2);
                if (qd == 7) pv = (u32x2){nosent(ubits(s1)), nosent(ubits(s2))};
                const unsigned nx = (unsigned)__builtin_amdgcn_update_dpp(0, (int)pv.x, 0xF5, 0xf, 0xf, true);
                const unsigned ny = (unsigned)__builtin_amdgcn_update_dpp(0, (int)pv.y, 0xF5, 0xf, 0xf, true);
                if (!(qd & 1)) {
                    const unsigned xo = (unsigned)((row >> 4) * 1024 + (qd >> 1) * 256 + (row & 15) * 16);
                    const unsigned xb = OFF_ST + L_TOTAL + L_XI + (unsigned)s * XI_SZ + (unsigned)v * 2048u;
                    st16(ws, xo, xb, (u32x4){pv.x, pv.y, nx, ny});
                    if (s + ARM_K < NSEG) st16(ws, xo, xb + ARM_K * XI_SZ, SENT4);
                }
            } else if (qd < 7) {
                *(u32x2*)(p.out + (size_t)(s * 32 + row) * DM + Q * 4) = pk4(r);
            }
            TR(6);
        }
    }
}

DI void role_wo(const Params& p, rsrc_t ws, int L, int y, unsigned char* lds) {
    constexpr unsigned OI_OFF = 0, ROWB = 912, RED_OFF = OI_OFF + 16 * ROWB, W_OFF = 16384;
    const int mt = y & 1;
    const int tid = threadIdx.x, w = __builtin_amdgcn_readfirstlane(tid >> 6), l = tid & 63, g = l >> 4, r16 = l & 15;
    const unsigned SB = OFF_ST + (unsigned)L * L_TOTAL;
    const u32x4* fw = (const u32x4*)(p.ws + OFF_FW);
    bf16x8 wr[3][14];
#pragma unroll
    for (int t = 0; t < 3; ++t)
#pragma unroll
        for (int kk = 0; kk < 14; ++kk) wr[t][kk] = as8(fw[((3 * w + t) * 14 + kk) * 64 + l]);
#pragma unroll
    for (int k = 0; k < 7; ++k) sts16(lds, W_OFF + (tid + 512 * k) * 16u, fw[24 * 14 * 64 + tid + 512 * k]);
    const int ntile = w < 4 ? 4 : 3;
    unsigned xio[4];
#pragma unroll
    for (int t = 0; t < 4; ++t) {
        const int nt = t < 3 ? 3 * w + t : 24 + (w & 3), Q = 4 * nt + g;
        xio[t] = (unsigned)((Q / 7) * 2048 + mt * 1024 + ((Q % 7) >> 1) * 256 + r16 * 16 + ((Q % 7) & 1) * 8);
    }
    unsigned xno[2], xmo[2];
#pragma unroll
    for (int pr = 0; pr < 2; ++pr) {
        const int t = 2 * pr + (g & 1), nt = t < 3 ? 3 * w + t : 24 + (w & 3);
        xno[pr] = (unsigned)(((nt >> 1) * 2 + mt) * 1024 + (nt & 1) * 512 + ((g >> 1) * 16 + r16) * 16);
        xmo[pr] = (unsigned)(nt * 1024 + (mt * 16 + r16) * 32 + (g >> 1) * 16);
    }
    const unsigned oo = (unsigned)(w * 2048 + mt * 1024 + l * 16);
    for (int s = (y >> 1) & 1; s < NSEG; s += 2) {
        TR(0);
        u32x4 ov[2]; u32x2 xr[4];
        {
            const unsigned ob = SB + L_O + (unsigned)s * O_SZ, xb = SB + L_XI + (unsigned)s * XI_SZ;
            for (unsigned spins = 0;;) {
                ov[0] = ld16(ws, oo, ob); ov[1] = ld16(ws, oo, ob + 16384u);
                bool ok = ok4(ov[0]) & ok4(ov[1]);
#pragma unroll
                for (int t = 0; t < 4; ++t) { xr[t] = ld8(ws, xio[t], xb); ok = ok & ok2(xr[t]); }
                if (__all(ok)) break;
                if (!retry(ws, ob + (unsigned)(w * 2048 + mt * 1024), spins)) return;
            }
        }
        TR(1);
#pragma unroll
        for (int k = 0; k < 2; ++k) {
            const int hh = w + 8 * k, row = (l >> 1) & 15, u4 = (l >> 5) * 2 + (l & 1);
            const unsigned d = OI_OFF + (unsigned)(row * ROWB + hh * 56 + u4 * 16);
            sts8(lds, d, (u32x2){ov[k].x, ov[k].y});
            if (u4 != 3) sts8(lds, d + 8, (u32x2){ov[k].z, ov[k].w});
        }
        __syncthreads();
        f32x4 acc[4];
#pragma unroll
        for (int t = 0; t < 4; ++t) acc[t] = ZERO4;
#define WOLOOP(FOUR) _Pragma("unroll") for (int kk = 0; kk < 14; ++kk) { \
            const bf16x8 ob = as8(lds16(lds, OI_OFF + (unsigned)(r16 * ROWB + kk * 64 + g * 16))); \
            acc[0] = mfma(wr[0][kk], ob, acc[0]); acc[1] = mfma(wr[1][kk], ob, acc[1]); acc[2] = mfma(wr[2][kk], ob, acc[2]); \
            if (FOUR) acc[3] = mfma(as8(lds16(lds, W_OFF + (unsigned)((w * 14 + kk) * 1024 + l * 16))), ob, acc[3]); }
        if (w < 4) { WOLOOP(true) } else { WOLOOP(false) }
        TR(2);
        f32x4 xm[4]; float sum = 0.f, sq = 0.f;
#pragma unroll
        for (int t = 0; t < 4; ++t) {
            xm[t][0] = bfr(bflo(xr[t].x) + bfr(acc[t][0])); xm[t][1] = bfr(bfhi(xr[t].x) + bfr(acc[t][1]));
            xm[t][2] = bfr(bflo(xr[t].y) + bfr(acc[t][2])); xm[t][3] = bfr(bfhi(xr[t].y) + bfr(acc[t][3]));
            if (t < ntile) {
                sum += (xm[t][0] + xm[t][1]) + (xm[t][2] + xm[t][3]);
                sq += __builtin_fmaf(xm[t][0], xm[t][0], xm[t][1] * xm[t][1]) + __builtin_fmaf(xm[t][2], xm[t][2], xm[t][3] * xm[t][3]);
            }
        }
        sum = xg_sum(sum); sq = xg_sum(sq);
        if (l < 16) sts8(lds, RED_OFF + (unsigned)(l * 64 + w * 8), (u32x2){ubits(sum), ubits(sq)});
        __syncthreads();
        float mean, rstd;
        {
            const f32x4 r0 = ldsf4(lds, RED_OFF + r16 * 64u), r1 = ldsf4(lds, RED_OFF + r16 * 64u + 16u),
                        r2 = ldsf4(lds, RED_OFF + r16 * 64u + 32u), r3 = ldsf4(lds, RED_OFF + r16 * 64u + 48u);
            const f32x4 rs = (r0 + r1) + (r2 + r3);
            mean = (rs[0] + rs[2]) * (1.0f / DM);
            rstd = __builtin_amdgcn_rsqf(fmaxf(__builtin_fmaf(-mean, mean, (rs[1] + rs[3]) * (1.0f / DM)), 0.f) + 1e-5f);
        }
#pragma unroll
        for (int pass = 0; pass < 2; ++pass)
#pragma unroll
            for (int pr = 0; pr < 2; ++pr) {
                const u32x2 a = pk4s(pass == 0 ? (xm[2 * pr] - mean) * rstd : xm[2 * pr]);
                const u32x2 b = pk4s(pass == 0 ? (xm[2 * pr + 1] - mean) * rstd : xm[2 * pr + 1]);
                const auto qx = __builtin_amdgcn_permlane16_swap(a.x, b.x, false, false), qy = __builtin_amdgcn_permlane16_swap(a.y, b.y, false, false);
                const u32x4 u = {qx[0], qy[0], qx[1], qy[1]};
                if (pr == 0 || w < 4 || !(g & 1))
                    st16(ws, pass == 0 ? xno[pr] : xmo[pr], SB + (pass == 0 ? L_XN : L_XMID) + (unsigned)s * XN_SZ, u);
            }
        if (s + ARM_K < NSEG) {
#pragma unroll
            for (int pr = 0; pr < 2; ++pr)
                if (pr == 0 || w < 4 || !(g & 1)) {
                    st16(ws, xno[pr], SB + L_XN + ((unsigned)s + ARM_K) * XN_SZ, SENT4);
                    st16(ws, xmo[pr], SB + L_XMID + ((unsigned)s + ARM_K) * XN_SZ, SENT4);
                }
        }
        TR(6);
    }
}

DI void role_at(const Params& p, rsrc_t ws, int L, int h, unsigned char* lds) {
    constexpr unsigned MGW = 2048 + 128;
    constexpr unsigned RB = 80, VRB = 1040, MG_OFF = 0, Q_OFF = 8 * MGW, ST_OFF = Q_OFF + 32 * RB, XA_OFF = ST_OFF + 256,
                       K_OFF = XA_OFF + 32768, V_OFF = K_OFF + 512 * RB;
    const int tid = threadIdx.x, w = __builtin_amdgcn_readfirstlane(tid >> 6), l = tid & 63, g = l >> 4, r16 = l & 15;
    const unsigned SB = OFF_ST + (unsigned)L * L_TOTAL;
    const u32x4* fq = (const u32x4*)(p.ws + OFF_FQ + (size_t)h * 6 * 16 * 1024);
    const int wt = w < 4 ? (w & 2) : 4 + (w & 1), wt2 = w < 4 ? wt + 1 : wt;
    bf16x8 wr[16], wq[16];
#pragma unroll
    for (int kk = 0; kk < 16; ++kk) { wr[kk] = as8(fq[(wt * 16 + kk) * 64 + l]); wq[kk] = as8(fq[(wt2 * 16 + kk) * 64 + l]); }
    f32x4 wsum2 = ZERO4;
    f32x4 wsum = ZERO4;
    {
        const bf16x8 one = as8((u32x4){0x3F803F80u, 0x3F803F80u, 0x3F803F80u, 0x3F803F80u});
#pragma unroll
        for (int kk = 0; kk < 16; ++kk) { wsum = w < 4 ? mfma(wr[kk], one, wsum) : mfma(one, wr[kk], wsum); wsum2 = mfma(wq[kk], one, wsum2); }
    }
    unsigned xo[4];
#pragma unroll
    for (int k = 0; k < 4; ++k) {
        const int pc = (w - 2) * 16 + k * 4 + g;
        xo[k] = (unsigned)(w < 2 ? ((4 * k + g) * 2 + w) * 1024 + 768 + r16 * 16 : (pc / 3) * 1024 + (pc % 3) * 256 + r16 * 16);
    }
    const int qt = w & 1, kq = w >> 1;
    const unsigned ooff = (w < 4 && !(g & 1)) ? (unsigned)((qt * 2 + (w >> 1)) * 512 + r16 * 32 + g * 8) : NOWHERE;
    const float scl = 0.18898223650461362f * LOG2E;
    u32x4 xv[4];
    const unsigned pro = (unsigned)((2 * w + (l & 1)) * 2048 + (l & 1) * 1024 + 752);
    bool got = false;
    unsigned pf = L == 0 ? 0u : SENT;
    u32x4 xa[4], xb_[4], xc[4];
#define CLD(d, en, src) do { const rsrc_t r_ = __builtin_amdgcn_make_buffer_rsrc(p.ws, 0, (en) ? WS_NEED : 0u, 0x00020000); \
        d[0] = ld16(r_, xo[0], src); d[1] = ld16(r_, xo[1], src); d[2] = ld16(r_, xo[2], src); d[3] = ld16(r_, xo[3], src); } while (0)
#define PROBE(src) pf = ld4(__builtin_amdgcn_make_buffer_rsrc(p.ws, 0, got ? 0u : WS_NEED, 0x00020000), pro, src)
#define POLL(d, src) do { bool en_ = false; if (!got) { en_ = none_sent(pf); got = en_; } CLD(d, en_, src); } while (0)
#pragma unroll
    for (int k = 0; k < 4; ++k) { xa[k] = (u32x4){0u, 0u, 0u, 0u}; xb_[k] = xa[k]; xc[k] = xa[k]; }

    for (int s = 0; s < NSEG; ++s) {
        TR(0);
        const unsigned xb = SB + L_XI + (unsigned)s * XI_SZ;
        {
            unsigned spins = 0;
#pragma unroll
            for (int k = 0; k < 4; ++k) xv[k] = xa[k] | xb_[k] | xc[k];
            const bool near_ = none_sent(pf);
            if (!got || !none_sent(max(max(mx4(xv[0]), mx4(xv[1])), max(mx4(xv[2]), mx4(xv[3]))))) {
                if (!near_)
                    for (;;) {
                        __builtin_amdgcn_s_sleep(AT_GAP);
                        if (none_sent(ld4(ws, pro, xb))) break;
                        if (!spin_ok(spins, ws)) return;
                    }
                for (;;) {
#pragma unroll
                    for (int k = 0; k < 4; ++k) xv[k] = ld16(ws, xo[k], xb);
                    if (none_sent(max(max(mx4(xv[0]), mx4(xv[1])), max(mx4(xv[2]), mx4(xv[3]))))) break;
                    if (!spin_ok(spins, ws)) return;
                }
            }
        }
        TR(1);
        if (w < 2) {
            const float s1 = xg_sum((fbits(xv[0].z) + fbits(xv[1].z)) + (fbits(xv[2].z) + fbits(xv[3].z)));
            const float s2 = xg_sum((fbits(xv[0].w) + fbits(xv[1].w)) + (fbits(xv[2].w) + fbits(xv[3].w)));
            const float mean = s1 * (1.0f / DM);
            const float var = fmaxf(__builtin_fmaf(-mean, mean, s2 * (1.0f / DM)), 0.f);
            const float rstd = __builtin_amdgcn_rsqf(var + 1e-5f);
            if (l < 16) sts8(lds, ST_OFF + (unsigned)(w * 16 + l) * 8u, (u32x2){ubits(rstd), ubits(-mean * rstd)});
#pragma unroll
            for (int k = 0; k < 4; ++k) { xv[k].z = 0u; xv[k].w = 0u; }
        }
#pragma unroll
        for (int k = 0; k < 4; ++k) sts16(lds, XA_OFF + xo[k], xv[k]);
        const unsigned xn = s + 1 < NSEG ? xb + XI_SZ : xb;
        got = L == 0;
        CLD(xa, got, xn);
        PROBE(xn);
        __syncthreads();
        TR(2);
        const int slot = s & 15;
        if (w < 4) {
            f32x4 acc[2];
            acc[0] = ZERO4; acc[1] = ZERO4;
            const unsigned mt = (unsigned)w & 1u;
            const u32x2 st0 = lds8(lds, ST_OFF + mt * 128u + (unsigned)r16 * 8u);
#pragma unroll
            for (int kk = 0; kk < 16; ++kk) {
                const bf16x8 xf = as8(lds16(lds, XA_OFF + mt * 1024u + (unsigned)(kk * 2048 + l * 16)));
                acc[0] = mfma(wr[kk], xf, acc[0]); acc[1] = mfma(wq[kk], xf, acc[1]);
            }
            const unsigned base = (w < 2 ? Q_OFF : K_OFF + (unsigned)slot * 32u * RB) + (mt * 16u + (unsigned)r16) * RB + (unsigned)g * 8u;
#pragma unroll
            for (int r = 0; r < 4; ++r) {
                acc[0][r] = __builtin_fmaf(acc[0][r], fbits(st0.x), wsum[r] * fbits(st0.y));
                acc[1][r] = __builtin_fmaf(acc[1][r], fbits(st0.x), wsum2[r] * fbits(st0.y));
            }
            sts8(lds, base, pk4(acc[0])); sts8(lds, base + 32u, pk4(acc[1]));
        } else {
            const unsigned mt = (unsigned)(w >> 1) & 1u;
            const f32x4 sa = ldsf4(lds, ST_OFF + mt * 128u + (unsigned)g * 32u), sb = ldsf4(lds, ST_OFF + mt * 128u + (unsigned)g * 32u + 16u);
            f32x4 acc = ZERO4;
#pragma unroll
            for (int kk = 0; kk < 16; ++kk)
                acc = mfma(as8(lds16(lds, XA_OFF + mt * 1024u + (unsigned)(kk * 2048 + l * 16))), wr[kk], acc);
            acc[0] = __builtin_fmaf(acc[0], sa[0], wsum[0] * sa[1]); acc[1] = __builtin_fmaf(acc[1], sa[2], wsum[0] * sa[3]);
            acc[2] = __builtin_fmaf(acc[2], sb[0], wsum[0] * sb[1]); acc[3] = __builtin_fmaf(acc[3], sb[2], wsum[0] * sb[3]);
            sts8(lds, V_OFF + (unsigned)(((w & 1) * 16 + r16) * VRB + slot * 64 + g * 16) + mt * 8u, pk4(acc));
        }
        __syncthreads();
        TR(3);
        POLL(xb_, xn);
        {
            constexpr float NEG = -3.0e38f;
            const bf16x8 qf = as8(lds16(lds, Q_OFF + (unsigned)((qt * 16 + r16) * RB + g * 16)));
            int qd = qt * 16 + r16 - 4 * g;
            asm volatile("" : "+v"(qd));
            const unsigned kl = K_OFF + (unsigned)(r16 * RB + g * 16), vl = V_OFF + (unsigned)(r16 * VRB + g * 16);
            f32x4 sc[2][2];
#pragma unroll
            for (int jj = 0; jj < 2; ++jj) {
                const int j = kq + 4 * jj;
                const unsigned sl = (unsigned)(s - j) & 15u;
#pragma unroll
                for (int kt = 0; kt < 2; ++kt) {
                    sc[jj][kt] = mfma(as8(lds16(lds, kl + (sl * 32u + kt * 16u) * RB)), qf, ZERO4);
                    if (j > s) sc[jj][kt] = (f32x4){NEG, NEG, NEG, NEG};
                }
            }
            if (kq == 0) {
                const unsigned sl = (unsigned)(s - 8) & 15u;
#pragma unroll
                for (int kt = 0; kt < 2; ++kt) {
                    f32x4 c = mfma(as8(lds16(lds, kl + (sl * 32u + kt * 16u) * RB)), qf, ZERO4);
                    if (s < 8) c = (f32x4){NEG, NEG, NEG, NEG};
#pragma unroll
                    for (int r = 0; r < 4; ++r) sc[0][kt][r] = qd >= kt * 16 + r ? sc[0][kt][r] : c[r];
                }
            }
            float mx = NEG;
#pragma unroll
            for (int jj = 0; jj < 2; ++jj)
#pragma unroll
                for (int kt = 0; kt < 2; ++kt)
                    mx = fmaxf(fmaxf(mx, fmaxf(sc[jj][kt][0], sc[jj][kt][1])), fmaxf(sc[jj][kt][2], sc[jj][kt][3]));
            mx = xg_max(mx) * scl;
            PROBE(xn);
            float sum = 0.f;
            f32x4 o[2];
            o[0] = ZERO4; o[1] = ZERO4;
#pragma unroll
            for (int jj = 0; jj < 2; ++jj) {
                const int j = kq + 4 * jj;
                const unsigned sl = (unsigned)(s - j) & 15u;
                f32x4 e[2];
#pragma unroll
                for (int kt = 0; kt < 2; ++kt) {
#pragma unroll
                    for (int r = 0; r < 4; ++r) e[kt][r] = __builtin_amdgcn_exp2f(__builtin_fmaf(sc[jj][kt][r], scl, -mx));
                    sum += (e[kt][0] + e[kt][1]) + (e[kt][2] + e[kt][3]);
                }
                if (jj == 0 && kq == 0) {
                    f32x4 e8[2];
#pragma unroll
                    for (int kt = 0; kt < 2; ++kt)
#pragma unroll
                        for (int r = 0; r < 4; ++r) {
                            const bool near_ = qd >= kt * 16 + r;
                            e8[kt][r] = near_ ? 0.f : e[kt][r]; e[kt][r] = near_ ? e[kt][r] : 0.f;
                        }
                    if (s >= 8) {
                        const unsigned s8 = (unsigned)(s - 8) & 15u;
                        const bf16x8 pf = cat8(pk4(e8[0]), pk4(e8[1]));
#pragma unroll
                        for (int dt = 0; dt < 2; ++dt) o[dt] = mfma(as8(lds16(lds, vl + dt * 16u * VRB + s8 * 64u)), pf, o[dt]);
                    }
                }
                if (j <= s) {
                    const bf16x8 pf = cat8(pk4(e[0]), pk4(e[1]));
#pragma unroll
                    for (int dt = 0; dt < 2; ++dt) o[dt] = mfma(as8(lds16(lds, vl + dt * 16u * VRB + sl * 64u)), pf, o[dt]);
                }
            }
            sum = kq > s ? 0.f : xg_sum(sum);
            const unsigned mb = MG_OFF + (unsigned)w * MGW;
            stsf4(lds, mb + l * 16u, o[0]); stsf4(lds, mb + 1024u + l * 16u, o[1]);
            if (l < 16) sts8(lds, mb + 2048u + l * 8u, (u32x2){ubits(mx), ubits(sum)});
        }
        POLL(xc, xn);
        PROBE(xn);
        __syncthreads();
        TR(4);
        u32x4 od = {0u, 0u, 0u, 0u};
        if (w < 4) {
            const int dt = w >> 1;
            float m4[4], l4[4]; float M = -3.0e38f;
            f32x4 pv[4];
#pragma unroll
            for (int k = 0; k < 4; ++k) {
                const u32x2 ml = lds8(lds, MG_OFF + (unsigned)(2 * k + qt) * MGW + 2048u + r16 * 8u);
                pv[k] = ldsf4(lds, MG_OFF + (unsigned)(2 * k + qt) * MGW + (unsigned)(dt * 1024 + l * 16));
                m4[k] = fbits(ml.x); l4[k] = fbits(ml.y); M = fmaxf(M, m4[k]);
            }
            float lsum = 0.f; f32x4 o = ZERO4;
#pragma unroll
            for (int k = 0; k < 4; ++k) {
                const float f = __builtin_amdgcn_exp2f(m4[k] - M);
                lsum += l4[k] * f;
                o += pv[k] * f;
            }
            o *= __builtin_amdgcn_rcpf(lsum);
            if (dt == 1 && g == 3) o = ZERO4;
            const u32x2 po = pk4s(o);
            const auto qx = __builtin_amdgcn_permlane16_swap(po.x, po.x, false, false), qy = __builtin_amdgcn_permlane16_swap(po.y, po.y, false, false);
            od = (u32x4){qx[0], qy[0], qx[1], qy[1]};
        }
        st16(ws, ooff, SB + L_O + (unsigned)s * O_SZ + (unsigned)h * 2048u, od);
        st16(ws, s + ARM_K < NSEG ? ooff : NOWHERE, SB + L_O + ((unsigned)s + ARM_K) * O_SZ + (unsigned)h * 2048u, SENT4);
        TR(6);
    }
}

__global__ void __launch_bounds__(NT, 2) k_main(Params p) {
    extern __shared__ __attribute__((aligned(16))) unsigned char lds[];
    const rsrc_t ws = __builtin_amdgcn_make_buffer_rsrc(p.ws, 0, WS_NEED, 0x00020000);
    const int b = blockIdx.x, x = b & 7, y = b >> 3;
#define LATE1 20000u
#define LATE0 0u
    if (LATE1 && (x == 1 || x == 5 || x == 6)) { const unsigned t = now(); while (now() - t < LATE1) __builtin_amdgcn_s_sleep(16); }
    if (LATE0 && x == 2) { const unsigned t = now(); while (now() - t < LATE0) __builtin_amdgcn_s_sleep(16); }
    if (x < 2) {
        if (y < 16) role_ms(p, ws, x, y, lds); else role_v(p, ws, x, y - 16, lds);
    } else {
        const int L = x >= 5 ? 1 : 0, z = x - 2 - 3 * L;
        if (z == 0) role_ts(p, ws, L, y, lds);
        else if (z == 1) { if (y < 28) role_vu(p, ws, L, y, lds); else role_wo(p, ws, L, y - 28, lds); }
        else { if (y < 16) role_at(p, ws, L, y, lds); else role_vo(p, ws, L, y - 16, lds); }
    }
}

__global__ void __launch_bounds__(NT, 2) k_pro(Params p) {
    extern __shared__ __attribute__((aligned(16))) unsigned char lds[];
    const int tid = threadIdx.x, w = __builtin_amdgcn_readfirstlane(tid >> 6), l = tid & 63, g = l >> 4, r16 = l & 15;
    const int bx = blockIdx.x & 7, by = blockIdx.x >> 3, b = by < 28 ? bx * 28 + by : 224 + bx * 4 + (by - 28);
    if (b < 224) {
        const bool isB = b >= 112;
        const int bb = isB ? b - 112 : b, h = bb / 7, nb = bb % 7;
        constexpr unsigned RS = 528;
        u32x4 bt[4];
#pragma unroll
        for (int k = 0; k < 4; ++k) {
            const int u = tid + 512 * k;
            bt[k] = isB ? *(const u32x4*)(p.Wo + ((size_t)h * 256 + (u >> 3)) * DM + nb * 64 + (u & 7) * 8)
                        : *(const u32x4*)(p.Wk + ((size_t)(nb * 64 + (u >> 5)) * 16 + h) * 256 + (u & 31) * 8);
        }
        u32x4 af[8][4];
        const int rot = (w + nb) & 3;
        if (w < 7) {
            const uint16_t* Wd = (isB ? p.Wv : p.Wq) + ((size_t)(w * 64 + ((r16 >> 2) & 1) * 8 + (r16 & 3)) * 16 + h) * 256 + (r16 >> 3) * 32 + g * 8;
#pragma unroll
            for (int ke = 0; ke < 8; ++ke)
#pragma unroll
                for (int i = 0; i < 4; ++i)
                    af[ke][i] = *(const u32x4*)(Wd + (size_t)((i >> 1) * 32 + (i & 1) * 4 + (ke & 1) * 16) * 4096 + (((ke >> 1) + rot) & 3) * 64);
        } else {
#pragma unroll
            for (int k = 0; k < 10; ++k) {
                const int q = min(b + 224 * k, NSEG * 32 - 1), mt = q & 1, v = (q >> 1) & 15, s = q >> 5;
                const uint16_t* src = p.x + (size_t)(s * 32 + mt * 16 + r16) * DM + v * 28 + g * 8;
                const u32x2 xa = *(const u32x2*)src, xc = *(const u32x2*)(src + (g == 3 ? 0 : 4));
                af[k >> 2][k & 3] = (u32x4){xa.x, xa.y, xc.x, xc.y};
            }
        }
#pragma unroll
        for (int k = 0; k < 4; ++k) {
            const int u = tid + 512 * k;
            if (isB) {
                const int e = u >> 3, c8 = u & 7;
#pragma unroll
                for (int j = 0; j < 4; ++j) {
                    *(uint16_t*)(lds + (c8 * 8 + 2 * j) * RS + e * 2) = (uint16_t)(bt[k][j] & 0xFFFFu);
                    *(uint16_t*)(lds + (c8 * 8 + 2 * j + 1) * RS + e * 2) = (uint16_t)(bt[k][j] >> 16);
                }
            } else sts16(lds, (u >> 5) * RS + (u & 31) * 16, bt[k]);
        }
        __syncthreads();
        if (w == 7) {
#pragma unroll
            for (int k = 0; k < 10; ++k) {
                const u32x2 xa = {af[k >> 2][k & 3].x, af[k >> 2][k & 3].y}, xc = {af[k >> 2][k & 3].z, af[k >> 2][k & 3].w};
                const float a0 = bflo(xa.x), a1 = bfhi(xa.x), a2 = bflo(xa.y), a3 = bfhi(xa.y);
                const float c0 = bflo(xc.x), c1 = bfhi(xc.x), c2 = bflo(xc.y), c3 = bfhi(xc.y);
                float s1 = (a0 + a1) + (a2 + a3), s2 = __builtin_fmaf(a0, a0, a1 * a1) + __builtin_fmaf(a2, a2, a3 * a3);
                if (g != 3) { s1 += (c0 + c1) + (c2 + c3); s2 += __builtin_fmaf(c0, c0, c1 * c1) + __builtin_fmaf(c2, c2, c3 * c3); }
                s1 = xg_sum(s1); s2 = xg_sum(s2);
                const u32x2 c = g == 3 ? (u32x2){ubits(s1), ubits(s2)} : xc;
                if (b + 224 * k < NSEG * 32)
                    *(u32x4*)(p.ws + OFF_ST + L_XI + (size_t)(b + 224 * k) * 1024 + l * 16) = (u32x4){nosent(xa.x), nosent(xa.y), nosent(c.x), nosent(c.y)};
            }
        }
        if (w < 7) {
            f32x4 acc[4][4];
#pragma unroll
            for (int i = 0; i < 4; ++i)
#pragma unroll
                for (int j = 0; j < 4; ++j) acc[i][j] = ZERO4;
#pragma unroll
            for (int ke = 0; ke < 8; ++ke) {
                if (!(ke & 1)) {
#pragma unroll
                    for (int i = 0; i < 4; ++i)
#pragma unroll
                        for (int c = 0; c < 4; ++c) {
                            const int x = (int)af[ke][i][c], y = (int)af[ke + 1][i][c];
                            af[ke][i][c] = (unsigned)__builtin_amdgcn_update_dpp(x, y, 0x128, 0xf, 0xC, false);
                            af[ke + 1][i][c] = (unsigned)__builtin_amdgcn_update_dpp(y, x, 0x128, 0xf, 0x3, false);
                        }
                }
#pragma unroll
                for (int j = 0; j < 4; ++j) {
                    const bf16x8 bfr_ = as8(lds16(lds, (unsigned)((j * 16 + r16) * RS + (((ke >> 1) + rot) & 3) * 128 + (ke & 1) * 64 + g * 16)));
#pragma unroll
                    for (int i = 0; i < 4; ++i) acc[i][j] = mfma(as8(af[ke][i]), bfr_, acc[i][j]);
                }
            }
            const float sc = isB ? 1.0f : 0.0625f;
#pragma unroll
            for (int pr = 0; pr < 2; ++pr)
#pragma unroll
                for (int j = 0; j < 4; ++j) {
                    const int nt = nb * 4 + j, kk = 2 * w + pr;
                    const size_t tile = isB ? (size_t)(nt * 16 + h) : (size_t)(h * 28 + nt);
                    const u32x2 lo = pk4(acc[2 * pr][j] * sc), hi = pk4(acc[2 * pr + 1][j] * sc);
                    *(u32x4*)(p.ws + (isB ? OFF_FB : OFF_FA) + (tile * 14 + kk) * 1024 + l * 16) = (u32x4){lo.x, lo.y, hi.x, hi.y};
                }
        }
        return;
    }
    const int gt = (b - 224) * NT + tid;
    uint16_t q[6][8], o[2][8];
#pragma unroll
    for (int k = 0; k < 6; ++k) {
        const int u = gt + k * 32 * NT, kk = (u >> 6) & 15, t = (u >> 10) % 6, h = u / 6144;
        const int cl = min((t & 1) * 16 + r16, 27);
        const uint16_t* src = p.wqkv + (size_t)(kk * 28 + g * 8) * 1344 + (t >> 1) * 448 + h * 28 + cl;
#pragma unroll
        for (int j = 0; j < 8; ++j) q[k][j] = src[(g == 3 ? (j & 3) : j) * 1344];
    }
#pragma unroll
    for (int k = 0; k < 2; ++k) {
        const int u = min(gt + k * 32 * NT, 28 * 14 * 64 - 64 + l), kk = (u >> 6) % 14, t = u / 896;
        const uint16_t* src = p.wo + (size_t)(kk * 32 + g * 8) * DM + t * 16 + r16;
#pragma unroll
        for (int j = 0; j < 8; ++j) o[k][j] = src[j * DM];
    }
    u32x2 m = {0u, 0u};
    const int mL = gt >= 896 ? 1 : 0, me = gt - mL * 896, mi = me & 7, mQ = me >> 3;
    if (gt < 2 * 896) m = *(const u32x2*)(p.mem0 + mi * DM + mQ * 4);
    asm volatile("" ::: "memory");
    for (unsigned u = gt; u < (OFF_FLG + SZ_FLG) / 16; u += 32 * NT) *(u32x4*)(p.ws + (size_t)u * 16) = (u32x4){0u, 0u, 0u, 0u};
#define ARM(off, bytes) _Pragma("unroll") for (unsigned u = 0; u < (bytes); u += 32u * NT * 16u) \
        if (u + gt * 16u < (bytes)) *(u32x4*)(ab + (off) + u) = SENT4
#pragma unroll
    for (int L = 0; L < 2; ++L) {
        unsigned char* ab = p.ws + OFF_ST + (size_t)L * L_TOTAL + gt * 16;
        ARM(L_MEM + MEM_SZ, 63u * MEM_SZ + 64u * PM_SZ);
        if (L == 1) ARM(L_XI, ARM_K * XI_SZ);
        ARM(L_O, ARM_K * O_SZ); ARM(L_XN, ARM_K * XN_SZ); ARM(L_XMID, ARM_K * XN_SZ); ARM(L_PT, ARM_K * PT_SZ); ARM(L_Y, ARM_K * Y_SZ);
    }
#pragma unroll
    for (int k = 0; k < 6; ++k) {
        const int u = gt + k * 32 * NT, t = (u >> 10) % 6;
        u32x4 v;
#pragma unroll
        for (int j = 0; j < 4; ++j) v[j] = (unsigned)q[k][2 * j] | ((unsigned)q[k][2 * j + 1] << 16);
        if (g == 3) { v[2] = 0u; v[3] = 0u; }
        if ((t & 1) * 16 + r16 >= 28) v = (u32x4){0u, 0u, 0u, 0u};
        *(u32x4*)(p.ws + OFF_FQ + (size_t)u * 16) = v;
    }
#pragma unroll
    for (int k = 0; k < 2; ++k) {
        const int u = gt + k * 32 * NT;
        u32x4 v;
#pragma unroll
        for (int j = 0; j < 4; ++j) v[j] = (unsigned)o[k][2 * j] | ((unsigned)o[k][2 * j + 1] << 16);
        if (u < 28 * 14 * 64) *(u32x4*)(p.ws + OFF_FW + (size_t)u * 16) = v;
    }
    if (gt < 2 * 896) {
        const int v = mQ / 7, qq = mQ % 7;
        unsigned char* d = p.ws + OFF_ST + (size_t)mL * L_TOTAL + L_MEM + v * 512 + mi * 8;
        *(u32x2*)(d + qq * 64) = (u32x2){nosent(m.x), nosent(m.y)};
        if (qq == 6) *(u32x2*)(d + 448) = (u32x2){0u, 0u};
    }
}

}

extern "C" void kernel_launch(void* const* d_in, const int* in_sizes, int n_in, void* d_out, int out_size, void* d_ws, size_t ws_size,
                              hipStream_t stream) {
    Params p;
    p.x = (const uint16_t*)d_in[0]; p.wqkv = (const uint16_t*)d_in[1]; p.wo = (const uint16_t*)d_in[2];
    p.mem0 = (const uint16_t*)d_in[4]; p.Wq = (const uint16_t*)d_in[5]; p.Wk = (const uint16_t*)d_in[6];
    p.Wv = (const uint16_t*)d_in[7]; p.Wo = (const uint16_t*)d_in[8];
    p.out = (uint16_t*)d_out; p.ws = (unsigned char*)d_ws;
    constexpr int LDS_PRO = 64 * 528;
    (void)hipFuncSetAttribute((const void*)k_main, hipFuncAttributeMaxDynamicSharedMemorySize, LDS_MAIN);
    hipLaunchKernelGGL(k_pro, dim3(256), dim3(NT), LDS_PRO, stream, p);
    hipLaunchKernelGGL(k_main, dim3(256), dim3(NT), LDS_MAIN, stream, p);
}
```
